```python
import torch

def _softmax(x, axis=-1):
    return torch.softmax(x, dim=axis)
def _gelu(x):
    return torch.nn.functional.gelu(x, approximate="tanh")
def _silu(x):
    return torch.nn.functional.silu(x)
def _sigmoid(x):
    return torch.sigmoid(x)
def _layernorm(x, axis=-1, eps=1e-5):
    m = x.mean(dim=axis, keepdim=True)
    v = x.var(dim=axis, keepdim=True, unbiased=False)
    return (x - m) / torch.sqrt(v + eps)
def _scatter_add(tgt, idx, src, axis=0):
    return tgt.clone().index_add_(axis, idx, src)
def _segment_sum(src, seg, n):
    out = torch.zeros((n,) + src.shape[1:], dtype=src.dtype)
    return out.index_add_(0, seg, src)
def _pad(x, pw):
    flat = tuple(v for pair in reversed(pw) for v in pair)
    return torch.nn.functional.pad(x, flat)

kernel_name = 'concat_heads__ssm_conv_s4__ssm_conv_h3__prompt'

def setup_inputs():

    _w = (lambda a: (lambda t: t.to(torch.bfloat16) if t.is_floating_point() else t)(torch.as_tensor(a)))
    def _A_setup():
        B, L, D, N = 1, 2048, 1024, 128
        A_re = -(lambda _x: torch.exp(torch.as_tensor(_x)))((lambda *s: torch.randn(*s, dtype=torch.bfloat16) * 3.2)(D, N))
        A_im = torch.pi * (lambda *s: torch.randn(*s, dtype=torch.bfloat16) * 0.05)(D, N)
        B_re = (lambda *s: torch.randn(*s, dtype=torch.bfloat16) * 0.1)(D, N)
        B_im = (lambda *s: torch.randn(*s, dtype=torch.bfloat16) * 0.1)(D, N)
        C_re = (lambda *s: torch.randn(*s, dtype=torch.bfloat16) * 0.1)(D, N)
        C_im = (lambda *s: torch.randn(*s, dtype=torch.bfloat16) * 0.1)(D, N)
        log_dt = (lambda *s: torch.randn(*s, dtype=torch.bfloat16) * 0.05)(D) * 0.5 - 4.0
        Dskip = (lambda *s: torch.randn(*s, dtype=torch.bfloat16) * 0.05)(D)
        return {
            "u": _w((lambda *s: torch.randn(*s, dtype=torch.bfloat16) * 0.05)(B, L, D)),
            "A_re": _w(A_re),
            "A_im": _w(A_im),
            "B_re": _w(B_re),
            "B_im": _w(B_im),
            "C_re": _w(C_re),
            "C_im": _w(C_im),
            "log_dt": _w(log_dt),
            "Dskip": _w(Dskip),
        }
    _A_in = _A_setup()

    def _B_setup():
        B, S, D = 1, 2048, 1024
        ks = 4
        return {
            "q": _w((lambda *s: torch.randn(*s, dtype=torch.bfloat16) * 0.05)(B, S, D)),
            "k": _w((lambda *s: torch.randn(*s, dtype=torch.bfloat16) * 0.2)(B, S, D)),
            "v": _w((lambda *s: torch.randn(*s, dtype=torch.bfloat16) * 0.2)(B, S, D)),
            "shift_k": _w((lambda *s: torch.randn(*s, dtype=torch.bfloat16) * 0.2)(D, ks) * 0.5),
            "ssm_k": _w((lambda *s: torch.randn(*s, dtype=torch.bfloat16) * 0.8)(D, S) * (1.0 / S ** 0.5)),
            "ssm_D": _w((lambda *s: torch.randn(*s, dtype=torch.bfloat16) * 0.2)(D)),
        }
    _B_in = _B_setup()

    return {
        "A_u": _A_in["u"],
        "A_A_re": _A_in["A_re"],
        "A_A_im": _A_in["A_im"],
        "A_B_re": _A_in["B_re"],
        "A_B_im": _A_in["B_im"],
        "A_C_re": _A_in["C_re"],
        "A_C_im": _A_in["C_im"],
        "A_log_dt": _A_in["log_dt"],
        "A_Dskip": _A_in["Dskip"],
        "B_q": _B_in["q"],
        "B_k": _B_in["k"],
        "B_v": _B_in["v"],
        "B_shift_k": _B_in["shift_k"],
        "B_ssm_k": _B_in["ssm_k"],
        "B_ssm_D": _B_in["ssm_D"],
    }

def reference(A_u, A_A_re, A_A_im, A_B_re, A_B_im, A_C_re, A_C_im, A_log_dt, A_Dskip, B_q, B_k, B_v, B_shift_k, B_ssm_k, B_ssm_D):
    def _A_ref(u, A_re, A_im, B_re, B_im, C_re, C_im, log_dt, Dskip):
        Bsz, L, D = u.shape
        dt = torch.exp(log_dt)[:, None]
        A = A_re + 1j * A_im
        Bc = B_re + 1j * B_im
        Cc = C_re + 1j * C_im
        logdA = dt * A
        dB = dt * Bc
        l = (lambda x, dt: x.to(dt))(torch.arange(L), A_re.dtype)
        P = torch.exp(logdA[:, :, None] * l[None, None, :])
        K = torch.einsum("dn,dnl,dn->dl", Cc, P, dB).real
        L2 = 2 * L
        Kp = _pad(K, ((0, 0), (0, L)))
        up = (lambda x, *d: x.permute(*d))(u, 0, 2, 1)
        up = _pad(up, ((0, 0), (0, 0), (0, L)))
        Kf = (lambda x, axes=(-2,-1): torch.fft.rfft2(x.float(), dim=axes))(Kp[:, None, :], axes=(-2, -1))
        Uf = (lambda x, axes=(-2,-1): torch.fft.rfft2(x.float(), dim=axes))(up[:, :, None, :], axes=(-2, -1))
        Yf = Uf * Kf[None, :, :, :]
        y = (lambda x, s, axes=(-2,-1): torch.fft.irfft2(x, s=s, dim=axes).to(torch.bfloat16))(Yf, (1, L2), axes=(-2, -1))[:, :, 0, :L]
        y = (lambda x, *d: x.permute(*d))(y, 0, 2, 1) + u * Dskip[None, None, :]
        return y

    def _B_ref(q, k, v, shift_k, ssm_k, ssm_D):
        B, S, D = q.shape
        ks = shift_k.shape[-1]
        k_pad = _pad(k, ((0, 0), (ks - 1, 0), (0, 0)))
        k_shift = torch.zeros_like(k)
        for t in range(ks):
            k_shift = k_shift + k_pad[:, t:t + S, :] * shift_k[:, ks - 1 - t][None, None, :]
        kv = k_shift * v
        L = 2 * S
        kv_pad = _pad(kv, ((0, 0), (0, S), (0, 0)))
        kf_pad = _pad(ssm_k, ((0, 0), (0, S)))
        kv_f = (lambda x, axes=(-2,-1): torch.fft.rfft2(x.float(), dim=axes))(kv_pad[:, None, :, :], axes=(1, 2))
        kf_f = (lambda x, axes=(-2,-1): torch.fft.rfft2(x.float(), dim=axes))(kf_pad[:, None, :], axes=(1, 2))
        kf_f = (lambda x, *d: x.permute(*d))(kf_f, 1, 2, 0)
        y_f = kv_f * kf_f[None, :, :, :]
        y = (lambda x, s, axes=(-2,-1): torch.fft.irfft2(x, s=s, dim=axes).to(torch.bfloat16))(y_f, (1, L), axes=(1, 2))[:, 0, :S, :]
        y = y + kv * ssm_D[None, None, :]
        return q * y

    a = _A_ref(A_u, A_A_re, A_A_im, A_B_re, A_B_im, A_C_re, A_C_im, A_log_dt, A_Dskip)
    b = _B_ref(B_q, B_k, B_v, B_shift_k, B_ssm_k, B_ssm_D)
    return torch.concatenate([a, b], axis=-1)
```

```cpp
#include <hip/hip_runtime.h>
#include <hip/hip_bf16.h>

typedef unsigned int u32;
typedef unsigned short u16;
typedef __attribute__((ext_vector_type(2))) u32 u32x2;
typedef __attribute__((ext_vector_type(4))) u32 u32x4;
typedef __attribute__((ext_vector_type(2))) float f32x2;
typedef __attribute__((ext_vector_type(4))) float f32x4;
typedef __attribute__((ext_vector_type(2))) __bf16 bf16x2;
typedef __attribute__((ext_vector_type(8))) __bf16 bf16x8;
typedef __attribute__((ext_vector_type(4))) short s16x4;
#define LDS __attribute__((address_space(3)))
typedef LDS char* lptr;

constexpr int XPH = 1152;
constexpr int XCH = 4624;
constexpr int XBYTES = 8 * XCH;
constexpr int WS = 9792;
constexpr int KD_OFF = 8448;
constexpr int SROW = 528;
constexpr int YROW = 144, YTILE = 576;
constexpr int LDS_BYTES = XBYTES + 8 * WS;

__device__ __forceinline__ __amdgpu_buffer_rsrc_t mkrsrc(const void* p, u32 bytes) {
    return __builtin_amdgcn_make_buffer_rsrc((void*)p, 0, bytes, 0x00020000);
}
__device__ __forceinline__ u32 pkbf(float lo, float hi) {
    f32x2 v = {lo, hi};
    return __builtin_bit_cast(u32, __builtin_convertvector(v, bf16x2));
}
__device__ __forceinline__ u32 pkbf_s(float lo, float hi) {
    u32 r;
    asm("v_cvt_pk_bf16_f32 %0, %1, %2" : "=v"(r) : "v"(lo), "v"(hi));
    return r;
}
__device__ __forceinline__ float lo16(u32 d) { return __builtin_bit_cast(float, d << 16); }
__device__ __forceinline__ float hi16(u32 d) { return __builtin_bit_cast(float, d & 0xffff0000u); }
__device__ __forceinline__ float bf2f(u16 h) { return __builtin_bit_cast(float, (u32)h << 16); }
__device__ __forceinline__ u32 perm_lo(u32 a, u32 b) { return __builtin_amdgcn_perm(b, a, 0x05040100u); }
__device__ __forceinline__ u32 perm_hi(u32 a, u32 b) { return __builtin_amdgcn_perm(b, a, 0x07060302u); }
__device__ __forceinline__ f32x2 cmul(f32x2 a, f32x2 z, f32x2 zs) { return a.yy * zs + a.xx * z; }
__device__ __forceinline__ f32x2 rot(f32x2 z) { return f32x2{-z.y, z.x}; }
__device__ __forceinline__ f32x2 cexp_rev(float e2, float rev) {
    float m = __builtin_amdgcn_exp2f(e2);
    float f = __builtin_amdgcn_fractf(rev);
    return f32x2{m * __builtin_amdgcn_cosf(f), m * __builtin_amdgcn_sinf(f)};
}
__device__ __forceinline__ u32x2 tr_read(lptr p) {
    return __builtin_bit_cast(u32x2, __builtin_amdgcn_ds_read_tr16_b64_v4i16((LDS s16x4*)p));
}
__device__ __forceinline__ f32x4 mfma(u32x4 a, u32x4 b, f32x4 c) {
    return __builtin_amdgcn_mfma_f32_16x16x32_bf16(__builtin_bit_cast(bf16x8, a), __builtin_bit_cast(bf16x8, b), c, 0, 0, 0);
}
__device__ __forceinline__ void wave_order() { __builtin_amdgcn_fence(__ATOMIC_ACQ_REL, "wavefront"); }

__device__ __forceinline__ void gen_kernel_a(lptr w, int lane, u32 are, u32 aim, u32 bre, u32 bim, u32 cre, u32 cim,
                                             float dt, float dskip) {
    f32x2 z1[2], z8[2], z256[2], wv[2];
#pragma unroll
    for (int p = 0; p < 2; ++p) {
        float ar = p ? hi16(are) : lo16(are), ai = p ? hi16(aim) : lo16(aim);
        float br = p ? hi16(bre) : lo16(bre), bi = p ? hi16(bim) : lo16(bim);
        float cr = p ? hi16(cre) : lo16(cre), ci = p ? hi16(cim) : lo16(cim);
        float e2 = (dt * ar) * 1.44269504088896341f;
        float rv = (dt * ai) * 0.15915494309189535f;
        z1[p] = cexp_rev(e2, rv);
        z8[p] = cexp_rev(e2 * 8.f, rv * 8.f);
        z256[p] = cexp_rev(e2 * 256.f, rv * 256.f);
        float dbr = dt * br, dbi = dt * bi;
        wv[p] = f32x2{cr * dbr - ci * dbi, -(cr * dbi + ci * dbr)};
        z1[p].y = -z1[p].y;
        z256[p].y = -z256[p].y;
    }
    lptr wrow = w + 8 * lane;
    const int g = lane >> 4;
    lptr rrow = w + (lane & 15) * SROW + 128 * (g >> 1) + 256 * (g & 1);

    u32x4 yf[2][8];
    f32x2 cur[2] = {f32x2{1.f, 0.f}, f32x2{1.f, 0.f}};
    f32x2 z8s[2] = {rot(z8[0]), rot(z8[1])};
#pragma unroll
    for (int h = 0; h < 2; ++h) {
#pragma unroll
        for (int r = 0; r < 16; ++r) {
            *(LDS u32x2*)(wrow + r * SROW) = u32x2{pkbf(cur[0].x, cur[0].y), pkbf(cur[1].x, cur[1].y)};
            if (h * 16 + r < 31) {
                cur[0] = cmul(cur[0], z8[0], z8s[0]);
                cur[1] = cmul(cur[1], z8[1], z8s[1]);
            }
        }
        wave_order();
#pragma unroll
        for (int k = 0; k < 8; ++k) yf[h][k] = *(LDS u32x4*)(rrow + 16 * k);
        wave_order();
    }

    f32x2 big[2][8];
    f32x2 row[2][16];
#pragma unroll
    for (int p = 0; p < 2; ++p) {
        f32x2 z1s = rot(z1[p]), z256s = rot(z256[p]);
        f32x2 z2 = cmul(z1[p], z1[p], z1s), z2s = rot(z2);
        f32x2 z4 = cmul(z2, z2, z2s), z4s = rot(z4);
        row[p][0] = wv[p];
#pragma unroll
        for (int a = 1; a < 8; ++a) row[p][a] = cmul(row[p][a - 1], z1[p], z1s);
        big[p][0] = cmul(wv[p], z256[p], z256s);
#pragma unroll
        for (int k = 1; k < 8; ++k) big[p][k] = cmul(big[p][k - 1], z256[p], z256s);
        row[p][8] = big[p][0];
        row[p][9] = cmul(row[p][8], z2, z2s);
        row[p][10] = cmul(row[p][8], z4, z4s);
        row[p][11] = cmul(row[p][10], z2, z2s);
        row[p][12] = big[p][1];
        row[p][13] = cmul(big[p][1], z4, z4s);
        row[p][14] = big[p][2];
        row[p][15] = cmul(big[p][2], z4, z4s);
    }
    u32 xr[2][21];
#pragma unroll
    for (int p = 0; p < 2; ++p)
#pragma unroll
        for (int r = 0; r < 21; ++r) {
            f32x2 v = r < 16 ? row[p][r] : big[p][r - 13];
            xr[p][r] = pkbf(v.x, v.y);
        }
    __builtin_amdgcn_sched_barrier(0);
    f32x4 c[2][2];
#pragma unroll
    for (int t = 0; t < 2; ++t) {
#pragma unroll
        for (int r = 0; r < (t ? 5 : 16); ++r) {
            u32 v0 = xr[0][16 * t + r], v1 = xr[1][16 * t + r];
            *(LDS u32x2*)(wrow + r * SROW) = u32x2{v0, v1};
        }
        wave_order();
        c[t][0] = f32x4{0.f, 0.f, 0.f, 0.f};
        c[t][1] = f32x4{0.f, 0.f, 0.f, 0.f};
#pragma unroll
        for (int k = 0; k < 8; ++k) {
            u32x4 xf = *(LDS u32x4*)(rrow + 16 * k);
            c[t][0] = mfma(xf, yf[0][k], c[t][0]);
            c[t][1] = mfma(xf, yf[1][k], c[t][1]);
        }
        wave_order();
    }
    if (lane == 0) c[0][0].x += dskip;

    lptr kd = w + KD_OFF;
    const int cc = lane & 15;
    const int i0 = g == 0 ? 8 * cc : g == 1 ? 8 * cc + 4 : g == 2 ? 256 + 4 * cc : 384 + 2 * cc;
    const int js = g < 2 ? 128 : g == 2 ? 64 : 32;
    const int d2 = g == 3 ? 64 : 2;
#pragma unroll
    for (int jt = 0; jt < 2; ++jt) {
        *(LDS u32*)(kd + 2 * (i0 + jt * js)) = pkbf(c[0][jt].x, c[0][jt].y);
        *(LDS u32*)(kd + 2 * (i0 + jt * js + d2)) = pkbf(c[0][jt].z, c[0][jt].w);
    }
    if (lane < 32) {
#pragma unroll
        for (int jt = 0; jt < 2; ++jt) {
            u32 p01 = pkbf(c[1][jt].x, c[1][jt].y), p23 = pkbf(c[1][jt].z, c[1][jt].w);
            lptr b = kd + 2 * (512 + 128 * g + cc + 16 * jt);
            *(LDS u16*)b = (u16)p01;
            if (lane < 16) {
                *(LDS u16*)(b + 64) = (u16)(p01 >> 16);
                *(LDS u16*)(b + 128) = (u16)p23;
                *(LDS u16*)(b + 192) = (u16)(p23 >> 16);
            }
        }
    }
    wave_order();
    u32x2 s0 = *(LDS u32x2*)(kd + 8 * lane);
    u32 a1 = *(LDS u32*)(kd + 512 + 4 * lane), b1 = *(LDS u32*)(kd + 516 + 4 * lane);
    u32 a2 = *(LDS u32*)(kd + 768 + 4 * lane), b2 = *(LDS u32*)(kd + 772 + 4 * lane);
    u32 a3 = *(LDS u32*)(kd + 1024 + 4 * lane), b3 = *(LDS u32*)(kd + 1028 + 4 * lane);
    wave_order();
    lptr kf = w + 32;
    if (lane < 2) *(LDS u32x4*)(w + 16 * lane) = u32x4{0, 0, 0, 0};
    *(LDS u32x2*)(kf + 8 * lane) = s0;
    *(LDS u32x2*)(kf + 512 + 8 * lane) = u32x2{perm_lo(a1, a1), __builtin_amdgcn_perm(b1, a1, 0x05040302u)};
    *(LDS u32x4*)(kf + 1024 + 16 * lane) = u32x4{perm_lo(a2, a2), a2, perm_hi(a2, a2), perm_lo(b2, b2)};
    u32 ee = perm_lo(a3, a3), oo = perm_hi(a3, a3), nn = perm_lo(b3, b3);
    *(LDS u32x4*)(kf + 2048 + 32 * lane) = u32x4{ee, ee, a3, oo};
    *(LDS u32x4*)(kf + 2064 + 32 * lane) = u32x4{oo, oo, nn, nn};
    wave_order();
}

template <int F>
__device__ __forceinline__ void x_read(u32 xa, u32x2& lo, u32x2& hi) {
    constexpr int off = (F & 3) * XPH + 64 * (F >> 2);
    asm volatile("ds_read_b64 %0, %2 offset:%3\n\tds_read_b64 %1, %2 offset:%4"
                 : "=&v"(lo), "=&v"(hi) : "v"(xa), "i"(off), "i"(off + 8) : "memory");
}
template <int F = 1>
__device__ __forceinline__ void x_read_n(int f, u32 xa, u32x2& lo, u32x2& hi) {
    if constexpr (F < 64) {
        if (f == F) x_read<F>(xa, lo, hi);
        else x_read_n<F + 1>(f, xa, lo, hi);
    }
}

__device__ __forceinline__ void conv_channel(lptr w, lptr xch, int lane) {
    const int g = lane >> 4, c = lane & 15;
    u32x4 T[16];
    {
        lptr ta = w + 2 * (112 - 32 * g - 4 * ((lane >> 2) & 3) + 4 * (lane & 3));
#pragma unroll
        for (int pi = 0; pi < 16; ++pi) {
            u32x2 lo = tr_read(ta + 256 * pi + 32), hi = tr_read(ta + 256 * pi);
            T[pi] = u32x4{lo.x, lo.y, hi.x, hi.y};
        }
    }
    __syncthreads();
    const u32 xa = (u32)(size_t)(xch + 8 + 8 * c + 16 * g);
    lptr yw = w + g * YROW + 8 * c;
    const int src = 4 * (lane >= 16 ? lane - 16 : (lane == 0 ? 63 : lane + 47));
    f32x4 acc[8][4];
    float prev0 = 0.f, prev1 = 0.f, prev2 = 0.f;
    u32x2 xl[2], xh[2];
    x_read<0>(xa, xl[0], xh[0]);
#pragma unroll
    for (int mu = -1; mu < 15; ++mu) {
#pragma unroll
        for (int rho = 0; rho < 4; ++rho) {
            const int f = 4 * (mu + 1) + rho;
            if (f < 63) {
                x_read_n(f + 1, xa, xl[(f + 1) & 1], xh[(f + 1) & 1]);
                asm volatile("s_waitcnt lgkmcnt(2)" : "+v"(xl[f & 1]), "+v"(xh[f & 1]));
            } else {
                asm volatile("s_waitcnt lgkmcnt(0)" : "+v"(xl[f & 1]), "+v"(xh[f & 1]));
            }
            u32x4 X = {xl[f & 1].x, xl[f & 1].y, xh[f & 1].x, xh[f & 1].y};
#pragma unroll
            for (int j = (mu + 1) >> 1; j < 8; ++j)
                acc[j][rho] = mfma(T[2 * j - mu], X, mu == -1 ? f32x4{0.f, 0.f, 0.f, 0.f} : acc[j][rho]);
        }
        if (mu >= 0 && (mu & 1) == 0) {
            const int j = mu >> 1;
            f32x4 a0 = acc[j][0], a1 = acc[j][1], a2 = acc[j][2], a3 = acc[j][3];
            float c0 = a1.w + a2.z + a3.y, c1 = a2.w + a3.z, c2 = a3.w;
            float v0 = __builtin_bit_cast(float, __builtin_amdgcn_ds_bpermute(src, __builtin_bit_cast(int, c0)));
            float v1 = __builtin_bit_cast(float, __builtin_amdgcn_ds_bpermute(src, __builtin_bit_cast(int, c1)));
            float v2 = __builtin_bit_cast(float, __builtin_amdgcn_ds_bpermute(src, __builtin_bit_cast(int, c2)));
            float o0 = a0.x + (lane == 0 ? prev0 : v0);
            float o1 = a0.y + a1.x + (lane == 0 ? prev1 : v1);
            float o2 = a0.z + a1.y + a2.x + (lane == 0 ? prev2 : v2);
            float o3 = a0.w + a1.z + a2.y + a3.x;
            prev0 = v0; prev1 = v1; prev2 = v2;
            *(LDS u32x2*)(yw + j * YTILE) = u32x2{pkbf_s(o0, o1), pkbf_s(o2, o3)};
        }
    }
}

__global__ void __launch_bounds__(512, 2)
ssm_conv_kernel(const u16* __restrict__ u, const u16* __restrict__ a_re, const u16* __restrict__ a_im,
                const u16* __restrict__ b_re, const u16* __restrict__ b_im, const u16* __restrict__ c_re,
                const u16* __restrict__ c_im, const u16* __restrict__ log_dt, const u16* __restrict__ dskip,
                const u16* __restrict__ q, const u16* __restrict__ k, const u16* __restrict__ v,
                const u16* __restrict__ shift_k, const u16* __restrict__ ssm_k, const u16* __restrict__ ssm_d,
                u16* __restrict__ out) {
    extern __shared__ __attribute__((aligned(16))) char smem[];
    lptr lds = (lptr)smem;
    const int tid = threadIdx.x, lane = tid & 63;
    const int wave = __builtin_amdgcn_readfirstlane(tid >> 6);
    const int slot = blockIdx.x >> 3;
    const bool is_b = slot < 16;
    const int grp = (blockIdx.x & 7) * 16 + (slot & 15);
    const int ch = grp * 8 + wave;
    lptr w = lds + XBYTES + wave * WS;
    const u32 ARR = 2048u * 1024u * 2u;

    if (tid < 256) *(LDS u32x4*)(lds + (tid >> 5) * XCH + ((tid >> 3) & 3) * XPH + 16 * (tid & 7)) = u32x4{0, 0, 0, 0};

    u32x4 qv[4];
    if (is_b) {
        auto rk = mkrsrc(k, ARR), rv = mkrsrc(v, ARR), rq = mkrsrc(q, ARR), rs = mkrsrc(ssm_k, ARR);
        u32x4 kk[4];
#pragma unroll
        for (int i = 0; i < 4; ++i) kk[i] = __builtin_amdgcn_raw_buffer_load_b128(rs, ch * 4096 + 16 * lane, 1024 * i, 0);
        const int rg = lane >> 2, cp = lane & 3;
        const u32 off = (u32)(256 * wave + 16 * rg) * 2048u + grp * 16 + cp * 4;
        u32 kr[19], vr[16];
#pragma unroll
        for (int i = 0; i < 3; ++i) kr[i] = __builtin_amdgcn_raw_buffer_load_b32(rk, off - 6144u, 2048 * i, 0);
#pragma unroll
        for (int i = 0; i < 16; ++i) {
            kr[3 + i] = __builtin_amdgcn_raw_buffer_load_b32(rk, off, 2048 * i, 0);
            vr[i] = __builtin_amdgcn_raw_buffer_load_b32(rv, off, 2048 * i, 0);
        }
#pragma unroll
        for (int i = 0; i < 4; ++i)
            qv[i] = __builtin_amdgcn_raw_buffer_load_b128(rq, (u32)(256 * wave + lane) * 2048u + grp * 16, 64 * 2048 * i, 0);
        u32x4 sk = *(const u32x4*)(shift_k + (grp * 8 + 2 * cp) * 4);
        const float dsk = bf2f(ssm_d[ch]);

        if (lane == 0) kk[0].x = (kk[0].x & 0xffff0000u) | (pkbf(lo16(kk[0].x) + dsk, 0.f) & 0xffffu);
        if (lane < 2) *(LDS u32x4*)(w + 16 * lane) = u32x4{0, 0, 0, 0};
#pragma unroll
        for (int i = 0; i < 4; ++i) *(LDS u32x4*)(w + 32 + 1024 * i + 16 * lane) = kk[i];

        lptr xw = lds + 2 * cp * XCH + 128 + 128 * wave + 8 * rg;
#pragma unroll
        for (int e = 0; e < 2; ++e) {
            const u32 s01 = e ? sk.z : sk.x, s23 = e ? sk.w : sk.y;
            const float w0 = lo16(s01), w1 = hi16(s01), w2 = lo16(s23), w3 = hi16(s23);
            float kf[19], kv[16];
#pragma unroll
            for (int i = 0; i < 19; ++i) kf[i] = e ? hi16(kr[i]) : lo16(kr[i]);
#pragma unroll
            for (int i = 0; i < 16; ++i) {
                float s = kf[i] * w3;
                s = fmaf(kf[i + 1], w2, s);
                s = fmaf(kf[i + 2], w1, s);
                s = fmaf(kf[i + 3], w0, s);
                kv[i] = s * (e ? hi16(vr[i]) : lo16(vr[i]));
            }
#pragma unroll
            for (int ph = 0; ph < 4; ++ph)
                *(LDS u32x2*)(xw + e * XCH + ph * XPH) = u32x2{pkbf(kv[ph], kv[ph + 4]), pkbf(kv[ph + 8], kv[ph + 12])};
        }
        wave_order();
    } else {
        const int po = ch * 64 + lane;
        u32 are = ((const u32*)a_re)[po], aim = ((const u32*)a_im)[po];
        u32 bre = ((const u32*)b_re)[po], bim = ((const u32*)b_im)[po];
        u32 cre = ((const u32*)c_re)[po], cim = ((const u32*)c_im)[po];
        auto ru = mkrsrc(u, ARR);
        const int ph = lane >> 4, wq = lane & 15;
        u32x4 ur[4];
#pragma unroll
        for (int i = 0; i < 4; ++i)
            ur[i] = __builtin_amdgcn_raw_buffer_load_b128(ru, (u32)(256 * wave + 16 * wq + ph) * 2048u + grp * 16, 4 * 2048 * i, 0);
        const float dt = lo16(pkbf(expf(bf2f(log_dt[ch])), 0.f));
        gen_kernel_a(w, lane, are, aim, bre, bim, cre, cim, dt, bf2f(dskip[ch]));
        lptr xw = lds + ph * XPH + 128 + 128 * wave + 8 * wq;
#pragma unroll
        for (int d = 0; d < 4; ++d) {
            *(LDS u32x2*)(xw + (2 * d) * XCH) = u32x2{perm_lo(ur[0][d], ur[1][d]), perm_lo(ur[2][d], ur[3][d])};
            *(LDS u32x2*)(xw + (2 * d + 1) * XCH) = u32x2{perm_hi(ur[0][d], ur[1][d]), perm_hi(ur[2][d], ur[3][d])};
        }
    }

    conv_channel(w, lds + wave * XCH, lane);
    __syncthreads();

    auto ro = mkrsrc(out, 2 * ARR);
    lptr yr = lds + XBYTES + ((lane >> 2) & 3) * WS + wave * YTILE + (lane & 3) * YROW + 8 * (lane >> 4);
    const u32 oo = (u32)(256 * wave + lane) * 4096u + (is_b ? 2048u : 0u) + grp * 16;
#pragma unroll
    for (int it = 0; it < 4; ++it) {
        u32x2 lo = tr_read(yr + 32 * it), hi = tr_read(yr + 32 * it + 4 * WS);
        u32x4 y = {lo.x, lo.y, hi.x, hi.y};
        if (is_b) {
#pragma unroll
            for (int d = 0; d < 4; ++d) y[d] = pkbf(lo16(y[d]) * lo16(qv[it][d]), hi16(y[d]) * hi16(qv[it][d]));
        }
        __builtin_amdgcn_raw_buffer_store_b128(y, ro, oo, 64 * 4096 * it, 0);
    }
}

extern "C" void kernel_launch(void* const* d_in, const int* in_sizes, int n_in, void* d_out, int out_size, void* d_ws,
                              size_t ws_size, hipStream_t stream) {
    (void)in_sizes; (void)n_in; (void)out_size; (void)d_ws; (void)ws_size;
    (void)hipFuncSetAttribute((const void*)ssm_conv_kernel, hipFuncAttributeMaxDynamicSharedMemorySize, LDS_BYTES);
    ssm_conv_kernel<<<256, 512, LDS_BYTES, stream>>>(
        (const u16*)d_in[0], (const u16*)d_in[1], (const u16*)d_in[2], (const u16*)d_in[3], (const u16*)d_in[4],
        (const u16*)d_in[5], (const u16*)d_in[6], (const u16*)d_in[7], (const u16*)d_in[8], (const u16*)d_in[9],
        (const u16*)d_in[10], (const u16*)d_in[11], (const u16*)d_in[12], (const u16*)d_in[13], (const u16*)d_in[14],
        (u16*)d_out);
}
```

```cpp
#include <hip/hip_runtime.h>

typedef unsigned int u32;
typedef unsigned short u16;
typedef __attribute__((ext_vector_type(2))) u32 u32x2;
typedef __attribute__((ext_vector_type(4))) u32 u32x4;
typedef __attribute__((ext_vector_type(16))) u32 u32x16;
typedef __attribute__((ext_vector_type(2))) float f32x2;
typedef __attribute__((ext_vector_type(4))) float f32x4;
typedef __attribute__((ext_vector_type(2))) __bf16 bf16x2;
typedef __attribute__((ext_vector_type(8))) __bf16 bf16x8;
typedef __attribute__((ext_vector_type(4))) short s16x4;
#define LDS __attribute__((address_space(3)))
typedef LDS char* lptr;

constexpr int XPH = 1152;
constexpr int XCH = 4624;
constexpr int XBYTES = 8 * XCH;
constexpr int WS_A = 8768, WS_B = 4672;
constexpr int K1_A = 2336;
constexpr int KD_OFF = 4688;
constexpr int SW_OFF = 4672;
constexpr int SROW = 528;
constexpr int EXT_A = 4 * SROW;
constexpr int FSTR = 168;
constexpr int YROW = 144, YTILE = 576;
constexpr int RAW_A = 2048, RAW_B = 2176;
constexpr int EXT_OFF = XBYTES + 8 * WS_A;
constexpr int EXT_IMM = 57344;
constexpr int JUNK = EXT_OFF + 8 * EXT_A + 16 * RAW_A;
constexpr int LDS_BYTES = JUNK + 256;
constexpr u32 ARR = 2048u * 1024u * 2u;
constexpr int TYPE_BITS = 31;
constexpr u32 NOWHERE = 0x40000000u;
constexpr u32 TOUCH_ARR = 0xfb4b4492u;
constexpr u32 TOUCH_STG = 0xedf80a54u;

#define SB() __builtin_amdgcn_sched_barrier(0)
#define ST(x) do { x; SB(); } while (0)
#define LW(n) ST(asm volatile("s_waitcnt lgkmcnt(" #n ")" ::: "memory"))
#define PIN(x) asm volatile("" : "+v"(x))
#define PRIO(p) ST(__builtin_amdgcn_s_setprio(p))

__device__ __forceinline__ u32x4 srd(const void* p, u32 bytes) {
    return u32x4{(u32)(size_t)p, (u32)((size_t)p >> 32), bytes, 0x00020000u};
}
__device__ __forceinline__ __amdgpu_buffer_rsrc_t mkrsrc(const void* p, u32 bytes) {
    return __builtin_amdgcn_make_buffer_rsrc((void*)p, 0, bytes, 0x00020000);
}
__device__ __forceinline__ u32 laddr(lptr p) { return (u32)(size_t)p; }
__device__ __forceinline__ void dma16(u32x4 r, u32 voff, u32 soff, u32 dst) {
    asm volatile("s_mov_b32 m0, %1\n\ts_nop 0\n\tbuffer_load_dwordx4 %0, %2, %3 offen lds"
                 :: "v"(voff), "s"(dst), "s"(r), "s"(soff) : "memory");
}
__device__ __forceinline__ void dma16_4lanes(u32x4 r, u32 voff, u32 dst) {
    asm volatile("s_mov_b32 m0, %1\n\ts_mov_b64 exec, 15\n\tbuffer_load_dwordx4 %0, %2, 0 offen lds\n\ts_mov_b64 exec, -1"
                 :: "v"(voff), "s"(dst), "s"(r) : "memory");
}
__device__ __forceinline__ void dma16_if(int half, int h, u32x4 r, u32 voff, u32 soff, u32 dst) {
    asm volatile("s_cmp_lg_u32 %4, %5\n\ts_cbranch_scc1 .Lskip%=\n\ts_mov_b32 m0, %1\n\ts_nop 0\n\t"
                 "buffer_load_dwordx4 %0, %2, %3 offen lds\n.Lskip%=:"
                 :: "v"(voff), "s"(dst), "s"(r), "s"(soff), "s"(half), "i"(h) : "memory", "scc");
}
__device__ __forceinline__ void dma4(u32x4 r, u32 voff, u32 dst) {
    asm volatile("s_mov_b32 m0, %1\n\ts_nop 0\n\tbuffer_load_dword %0, %2, 0 offen lds"
                 :: "v"(voff), "s"(dst), "s"(r) : "memory");
}
__device__ __forceinline__ u32x4 ld128(u32x4 r, u32 voff, u32 soff) {
    u32x4 x;
    asm volatile("buffer_load_dwordx4 %0, %1, %2, %3 offen" : "=v"(x) : "v"(voff), "s"(r), "s"(soff) : "memory");
    return x;
}
#define VMWAIT(n) asm volatile("s_waitcnt vmcnt(" #n ")" ::: "memory")

__device__ __forceinline__ u32 pkbf(float lo, float hi) {
    f32x2 v = {lo, hi};
    return __builtin_bit_cast(u32, __builtin_convertvector(v, bf16x2));
}
__device__ __forceinline__ u32 pkbf_s(float lo, float hi) {
    asm("v_cvt_pk_bf16_f32 %0, %0, %1" : "+v"(lo) : "v"(hi));
    return __builtin_bit_cast(u32, lo);
}
__device__ __forceinline__ float lo16(u32 d) { return __builtin_bit_cast(float, d << 16); }
__device__ __forceinline__ float hi16(u32 d) { return __builtin_bit_cast(float, d & 0xffff0000u); }
__device__ __forceinline__ u32 perm_lo(u32 a, u32 b) { return __builtin_amdgcn_perm(b, a, 0x05040100u); }
__device__ __forceinline__ u32 perm_hi(u32 a, u32 b) { return __builtin_amdgcn_perm(b, a, 0x07060302u); }
__device__ __forceinline__ u32 mix(u32 a, u32 b) { return __builtin_amdgcn_perm(b, a, 0x05040302u); }
__device__ __forceinline__ f32x2 cexp_rev(float e2, float rev) {
    float m = __builtin_amdgcn_exp2f(e2);
    float f = __builtin_amdgcn_fractf(rev);
    return f32x2{m * __builtin_amdgcn_cosf(f), m * __builtin_amdgcn_sinf(f)};
}
__device__ __forceinline__ u32x2 tr_read(lptr p) {
    return __builtin_bit_cast(u32x2, __builtin_amdgcn_ds_read_tr16_b64_v4i16((LDS s16x4*)p));
}
__device__ __forceinline__ f32x4 mfma(u32x4 a, u32x4 b, f32x4 c) {
    return __builtin_amdgcn_mfma_f32_16x16x32_bf16(__builtin_bit_cast(bf16x8, a), __builtin_bit_cast(bf16x8, b), c, 0, 0, 0);
}
__device__ __forceinline__ void wave_order() { __builtin_amdgcn_fence(__ATOMIC_ACQ_REL, "wavefront"); }

struct c2 { f32x2 p, q; };
__device__ __forceinline__ c2 cmulc(c2 a, c2 z) {
    c2 r;
    asm("v_pk_mul_f32 %0, %2, %4 op_sel_hi:[0,1] neg_hi:[0,1]\n\t"
        "v_pk_mul_f32 %1, %3, %5 op_sel_hi:[0,1] neg_hi:[0,1]\n\t"
        "v_pk_fma_f32 %0, %2, %4, %0 op_sel:[1,1,0] op_sel_hi:[1,0,1]\n\t"
        "v_pk_fma_f32 %1, %3, %5, %1 op_sel:[1,1,0] op_sel_hi:[1,0,1]"
        : "=&v"(r.p), "=&v"(r.q) : "v"(a.p), "v"(a.q), "v"(z.p), "v"(z.q));
    return r;
}
#define ROW_STEP(NM, NF) \
    asm volatile("v_pk_mul_f32 %0, %4, %6 op_sel_hi:[0,1]" NM "\n\t" \
                 "v_pk_mul_f32 %1, %5, %7 op_sel_hi:[0,1]" NM "\n\t" \
                 "v_cvt_pk_bf16_f32 %2, %8, %9\n\t" \
                 "ds_write_addtid_b32 %2 offset:%12\n\t" \
                 "v_pk_fma_f32 %0, %4, %6, %0 op_sel:[1,1,0] op_sel_hi:[1,0,1]" NF "\n\t" \
                 "v_pk_fma_f32 %1, %5, %7, %1 op_sel:[1,1,0] op_sel_hi:[1,0,1]" NF "\n\t" \
                 "v_cvt_pk_bf16_f32 %3, %10, %11\n\t" \
                 "ds_write_addtid_b32 %3 offset:%13" \
                 : "=&v"(r.p), "=&v"(r.q), "=&v"(d0), "=&v"(d1) \
                 : "v"(a.p), "v"(a.q), "v"(z.p), "v"(z.q), "v"(v.p.x), "v"(v.p.y), "v"(v.q.x), "v"(v.q.y), "i"(OFF), "i"(OFF + 256) \
                 : "memory")
template <int OFF, bool CONJ>
__device__ __forceinline__ c2 row_step(c2 v, c2 a, c2 z) {
    c2 r;
    u32 d0, d1;
    if constexpr (CONJ) ROW_STEP(" neg_hi:[0,1]", "");
    else ROW_STEP("", " neg_lo:[0,1,0]");
    SB();
    return r;
}
template <int OFF>
__device__ __forceinline__ void put_row(u32 d0, u32 d1) {
    ST(asm volatile("ds_write_addtid_b32 %0 offset:%2\n\tds_write_addtid_b32 %1 offset:%3" :: "v"(d0), "v"(d1), "i"(OFF), "i"(OFF + 256) : "memory"));
}
__device__ __forceinline__ void set_m0(u32 x) { ST(asm volatile("s_mov_b32 m0, %0\n\ts_nop 0" :: "s"(x) : "memory")); }
template <int K0 = 0>
__device__ __forceinline__ void get_frags(u32 addr, u32x4 (&f)[8]) {
    if constexpr (K0 < 8) {
        ST(asm volatile("ds_read_b128 %0, %1 offset:%2" : "=v"(f[K0]) : "v"(addr), "i"(16 * K0) : "memory"));
        get_frags<K0 + 1>(addr, f);
    }
}
template <int BASE, int R, int N, bool CONJ>
__device__ __forceinline__ c2 chain(c2 cur, c2 z, c2* keep) {
    if constexpr (R < N) {
        if (keep) keep[R] = cur;
        return chain<BASE, R + 1, N, CONJ>(row_step<BASE + R * SROW, CONJ>(cur, cur, z), z, keep);
    } else {
        return cur;
    }
}
template <int CTRL>
__device__ __forceinline__ float dpp_add(float x) {
    return x + __builtin_bit_cast(float, __builtin_amdgcn_update_dpp(0, __builtin_bit_cast(int, x), CTRL, 0xf, 0xf, true));
}

struct GenAddr {
    lptr w, ext;
    u32 mw, me, wa, ra, re;
    lptr k0, k1, k2, k3, head, zf, zk, l4;
    __device__ __forceinline__ GenAddr(lptr w_, lptr ext_, int lane) : w(w_), ext(ext_) {
        mw = laddr(w) - XBYTES;
        me = laddr(ext) - EXT_IMM;
        const int g = lane >> 4, cl = lane & 15, g8 = 8 * g;
        lptr kd = w + KD_OFF;
        wa = laddr(w) + 8 * lane;
        const u32 kslot = (lane & 32) * 4 + (lane & 16) * 16;
        ra = laddr(w) + cl * SROW + kslot;
        re = laddr(ext) + (cl & 3) * SROW + kslot;
        const u32 sh = __builtin_amdgcn_ubfe(0x02030404u, g8, 8), d2 = g == 3 ? 128 : 4;
        k0 = kd + (cl << sh) + 8 * __builtin_amdgcn_ubfe(0x60400100u, g8, 8);
        k1 = k0 + d2;
        k2 = k0 + (16 << sh);
        k3 = k2 + d2;
        head = lane == 63 ? kd - 8 : kd + 1056;
        zf = ext + (lane >> 3) * FSTR + 8 + 4 * (lane & 7);
        zk = w + (lane & 8 ? K1_A : 0) + 4 * (lane & 7);
        l4 = kd + 508 + 4 * lane;
        PIN(wa); PIN(ra); PIN(re); PIN(k0); PIN(k1); PIN(k2); PIN(k3); PIN(head); PIN(zf); PIN(zk); PIN(l4);
    }
};

template <class F0, class F1, class F2, class F3, class F4>
__device__ __forceinline__ void gen_kernel_a(const GenAddr& A, int lane, u32 are, u32 aim, u32 bre, u32 bim, u32 cre, u32 cim,
                                             float dt, float dskip, F0 load_rows, F1 read_rows, F2 write_rows, F3 shadow, F4 first_frags,
                                             f32x4& far0, f32x4& far1) {
    const float se = dt * 1.44269504088896341f, sr = dt * 0.15915494309189535f;
    const float e0 = se * lo16(are), e1 = se * hi16(are), r0 = sr * lo16(aim), r1 = sr * hi16(aim);
    const c2 z8 = {cexp_rev(e0 * 8.f, r0 * 8.f), cexp_rev(e1 * 8.f, r1 * 8.f)};
    const c2 z1 = {cexp_rev(e0, r0), cexp_rev(e1, r1)};
    const u32 ncim = cim ^ 0x80008000u;
    const c2 cc = {f32x2{lo16(cre), lo16(ncim)}, f32x2{hi16(cre), hi16(ncim)}};
    const c2 db = {f32x2{dt * lo16(bre), dt * lo16(bim)}, f32x2{dt * hi16(bre), dt * hi16(bim)}};
    const c2 wv = cmulc(cc, db);
    float ksum = dpp_add<0x143>(dpp_add<0x142>(dpp_add<0x118>(dpp_add<0x114>(dpp_add<0x112>(dpp_add<0x111>(wv.p.x + wv.q.x))))));
    const u32 wa = A.wa, ra = A.ra, re = A.re;
    SB();

    u32x4 yf[2][8], xf[2][8];
    set_m0(A.mw);
    put_row<XBYTES>(0x3f80u, 0x3f80u);
    c2 cur = chain<XBYTES, 1, 16, false>(z8, z8, nullptr);
    get_frags(ra, yf[0]);
    load_rows();
    set_m0(A.mw);
    PRIO(2);
    cur = chain<XBYTES, 0, 16, false>(cur, z8, nullptr);
    get_frags(ra, yf[1]);
    const c2 z256 = cur;
    PRIO(1);

    c2 low[8], a, b, c, d;
    ST(cur = cmulc(wv, z1));
    cur = chain<XBYTES, 0, 7, true>(cur, z1, low);
    a = row_step<XBYTES + 7 * SROW, true>(cur, low[1], z256);
    b = row_step<XBYTES + 8 * SROW, true>(a, low[3], z256);
    c = row_step<XBYTES + 9 * SROW, true>(b, low[5], z256);
    d = row_step<XBYTES + 10 * SROW, true>(c, cur, z256);
    a = row_step<XBYTES + 11 * SROW, true>(d, b, z256);
    c = row_step<XBYTES + 12 * SROW, true>(a, d, z256);
    b = row_step<XBYTES + 13 * SROW, true>(c, a, z256);
    d = row_step<XBYTES + 14 * SROW, true>(b, c, z256);
    a = row_step<XBYTES + 15 * SROW, true>(d, d, z256);
    get_frags(ra, xf[0]);
    set_m0(A.me);
    a = chain<EXT_IMM, 0, 3, true>(a, z256, nullptr);
    put_row<EXT_IMM + 3 * SROW>(pkbf_s(a.p.x, a.p.y), pkbf_s(a.q.x, a.q.y));
    get_frags(re, xf[1]);
    PRIO(0);
    read_rows();
    f32x4 m[2][2];
#define GM(t, k) do { ST(m[t][0] = mfma(xf[t][k], yf[0][k], k ? m[t][0] : f32x4{0.f, 0.f, 0.f, 0.f})); \
                      ST(m[t][1] = mfma(xf[t][k], yf[1][k], k ? m[t][1] : f32x4{0.f, 0.f, 0.f, 0.f})); } while (0)
    lptr w8 = (lptr)(size_t)wa, l8 = w8 + KD_OFF - 8, l16, fix;
    LW(14);
    GM(0, 0);
    ST(l16 = A.w + 16 * lane; PIN(l16));
    GM(0, 1);
    ST(fix = lane == 63 ? A.w + K1_A + 544 : A.w + KD_OFF + 1056; PIN(fix));
    GM(0, 2);
    LW(0);
    write_rows();
    GM(0, 3);
    ST(*(LDS u32x2*)A.head = (u32x2{0u, pkbf(0.f, ksum + dskip)}));
    GM(0, 4);
    ST(*(LDS u32*)A.zf = 0);
    GM(0, 5);
    ST(*(LDS u32*)A.zk = 0);
    GM(0, 6);
    shadow(2);
    GM(0, 7);
    shadow(3);
    GM(1, 0);
    shadow(0);
    GM(1, 1);
    ST(*(LDS u32*)A.k0 = pkbf(m[0][0].x, m[0][0].y); *(LDS u32*)A.k1 = pkbf(m[0][0].z, m[0][0].w));
    GM(1, 2);
    ST(*(LDS u32*)A.k2 = pkbf(m[0][1].x, m[0][1].y); *(LDS u32*)A.k3 = pkbf(m[0][1].z, m[0][1].w));
    wave_order();
    const u32x2 sp = *(LDS u32x2*)l8, s0 = *(LDS u32x2*)(l8 + 8);
    const u32 a1 = *(LDS u32*)A.l4, b1 = *(LDS u32*)(A.l4 + 4);
    const u32 a2 = *(LDS u32*)(A.l4 + 256), b2 = *(LDS u32*)(A.l4 + 260);
    wave_order();
    SB();
    GM(1, 3);
    shadow(1);
    GM(1, 4);
    GM(1, 5);
    GM(1, 6);
    GM(1, 7);
    const u32 fa = mix(sp.y, s0.x), fb = mix(s0.x, s0.y), e1g = perm_hi(a1, a1);
    const u32 e2 = perm_hi(a2, a2), eo2 = mix(a2, b2), o2 = perm_lo(b2, b2);
    ST(*(LDS u32x2*)(w8 + 32) = (u32x2{fa, fb}));
    ST(*(LDS u32x2*)(w8 + 544) = (u32x2{e1g, b1}));
    ST(*(LDS u32x4*)(l16 + 1056) = (u32x4{e2, eo2, o2, perm_hi(b2, b2)}));
    first_frags();
    ST(*(LDS u32x2*)(w8 + K1_A + 32) = (u32x2{mix(sp.x, sp.y), fa}));
    ST(*(LDS u32x2*)(w8 + K1_A + 544) = (u32x2{a1, e1g}));
    ST(*(LDS u32x4*)(l16 + K1_A + 1056) = (u32x4{e2, e2, eo2, o2}));
    wave_order();
    *(LDS u32*)fix = fb;
    *(LDS u32*)(fix + 512) = b1;
    wave_order();
    far0 = m[1][0];
    far1 = m[1][1];
}

struct Args {
    const u16 *u, *a_re, *a_im, *b_re, *b_im, *c_re, *c_im, *log_dt, *dskip, *q, *k, *v, *shift_k, *ssm_k, *ssm_d;
    u16* out;
};

__device__ __forceinline__ int wg_slot() {
    const int s = blockIdx.x >> 3;
    return (s >> 1) | (((__builtin_popcount(s & TYPE_BITS) & 1) ^ 1) << 4);
}
__device__ __forceinline__ int column(int second) { return (blockIdx.x & 7) + 8 * second; }
__device__ __forceinline__ u32x4 touch_srd(const Args& a, int n, bool on) {
    const int ai = (TOUCH_ARR >> (2 * n)) & 3;
    const size_t kv = ai & 1 ? (size_t)a.v : (size_t)a.k, uq = ai & 1 ? (size_t)a.q : (size_t)a.u;
    return srd((const void*)(ai & 2 ? uq : kv), on ? ARR : 0u);
}
__device__ __forceinline__ u32 touch_off(int n) {
    const int l = (wg_slot() & 15) * 64 + (threadIdx.x & 63);
    return (u32)(512 * ((TOUCH_STG >> (2 * n)) & 3) + (l >> 1)) * 2048u + column(l & 1) * 128;
}

template <bool IS_B>
__device__ __forceinline__ void run(const Args& a, lptr lds, int grp, u32 h0, u32 dd, u32 (&p)[6]) {
    constexpr int WS = IS_B ? WS_B : WS_A, RAW = IS_B ? RAW_B : RAW_A;
    const int tid = threadIdx.x, lane = tid & 63;
    const int wave = __builtin_amdgcn_readfirstlane(tid >> 6);
    const int half = wave >> 2;
    const int g = lane >> 4, c = lane & 15;
    lptr w = lds + XBYTES + wave * WS;
    lptr raw = lds + XBYTES + 8 * WS + (IS_B ? wave * 4 * RAW + 128 : 8 * EXT_A + wave * 2 * RAW);
    lptr w1 = IS_B ? raw + 2 * RAW - 128 : w + K1_A;

    u32 unit_voff = (u32)(IS_B ? 64 * wave + (lane ^ ((lane >> 2) & 4)) : 128 * wave + (lane & 60) + ((lane - g) & 3)) * 2048u + grp * 16;
    u32 halo_voff = (u32)(64 * wave + lane + 508) * 2048u + grp * 16;
    const u32x4 rx0 = srd(IS_B ? a.k : a.u, ARR), rx1 = srd(a.v, ARR);
    auto unit_piece = [&](int r, int k) {
        if constexpr (IS_B) {
            if (k < 2) dma16(k ? rx1 : rx0, unit_voff, r * 512 * 2048, laddr(raw) + r * RAW + k * 1024);
            else dma16_4lanes(rx0, halo_voff + (r == 0 && wave == 0 ? NOWHERE : (r - 1) * 512u * 2048u), laddr(raw) + r * RAW - 128);
        } else {
            dma16(rx0, unit_voff, (r * 1024 + k * 64) * 2048, laddr(raw) + r * RAW + k * 1024);
        }
    };

    if (tid < 256) *(LDS u32x4*)(lds + (tid >> 5) * XCH + ((tid >> 3) & 3) * XPH + 16 * (tid & 7)) = u32x4{0, 0, 0, 0};
    if constexpr (IS_B) {
        *(LDS u32*)(lane < 8 ? w + 4 * lane : w1 + 4 * (lane & 7)) = 0;
        __builtin_amdgcn_s_barrier();
        unit_piece(0, 2); unit_piece(0, 0); unit_piece(0, 1);
    }
    SB();

    u32x2 tl[2][16], th[2][16];
    u32 ta[2];
#define TR(d, s, off) ST(asm volatile("ds_read_b64_tr_b16 %0, %1 offset:%2" : "=v"(d) : "v"(ta[s]), "i"(off) : "memory"))

    const int rs = 2 * g + ((lane & 3) >> 1), rq4 = (lane >> 2) & 3;
    lptr xw = lds + (4 * ((c >> 2) & 1) + (c & 3)) * XCH + 128 + (IS_B ? 32 * wave + 4 * (2 * g + (c >> 3)) : 64 * wave + 8 * (2 * g + (c >> 3)));
    u32 pa[4];
#pragma unroll
    for (int i = 0; i < 4; ++i) {
        const int row = 8 * rs + 4 * (i - 1) + rq4;
        pa[i] = laddr(raw) + 8 * (lane & 1) + 16 * (IS_B ? row ^ ((row >> 2) & 4) : 16 * rs + 4 * rq4 + ((i + rs) & 3));
    }
    u32x2 pr[4], pk[3], pv[2];
    u32 px;
    float kf[12], fo[8], fv;
    float w0, w1f, w2, w3;
#define TRD(d, a, off) ST(asm volatile("ds_read_b64_tr_b16 %0, %1 offset:%2" : "=v"(d) : "v"(a), "i"(off) : "memory"))
#define PR(n, ph) TRD(pr[ph], pa[ph], (n) * RAW)
#define PW(n, ph) ST(*(LDS u32x2*)(xw + (ph) * XPH + (n) * 512) = pr[ph])
#define PK(r, a) TRD(pk[a], pa[a], (r) * RAW)
#define PV(r, a) TRD(pv[a], pa[(a) + 1], (r) * RAW + 1024)
#define HALF(d, i) ((i) & 1 ? hi16(d[((i) >> 1) & 1]) : lo16(d[((i) >> 1) & 1]))
#define UK(i) ST(kf[i] = HALF(pk[(i) >> 2], i))
#define UV(m) ST(fv = HALF(pv[(m) >> 2], m))
#define F0(m) ST(fo[m] = kf[(m) + 1] * w3)
#define F1(m) ST(fo[m] = fmaf(kf[(m) + 2], w2, fo[m]))
#define F2(m) ST(fo[m] = fmaf(kf[(m) + 3], w1f, fo[m]))
#define F3(m) ST(fo[m] = fmaf(kf[(m) + 4], w0, fo[m]))
#define FV(m) ST(fo[m] *= fv)
#define PC(ph) ST(px = pkbf_s(fo[ph], fo[(ph) + 4]))
#define PX(r, ph) ST(*(LDS u32*)(xw + (ph) * XPH + (r) * 256) = px)

    const u32x4 rq = srd(a.q, ARR);
    const auto ro = mkrsrc(a.out, 2 * ARR);
    u32 qoff, ooff, xa, ya, src, sa, faa, fba;
    lptr yw, sw;
    const bool last = lane == 63;
    auto addr_t = [&] {
        const u32 tofs = 224 - 64 * g - 8 * rq4 + 8 * (lane & 3);
        ta[0] = laddr(w) + tofs; ta[1] = laddr(w1) + tofs;
        PIN(ta[0]); PIN(ta[1]);
    };
    auto addr_x = [&] { xa = laddr(lds + wave * XCH + 8 + 8 * c + 16 * g); PIN(xa); };
    auto addr_y = [&] { yw = w + g * YROW + 8 * c; PIN(yw); };
    auto addr_c = [&] { src = 4 * (lane >= 16 ? lane - 16 : (lane == 0 ? 63 : lane + 47)); PIN(src); };
    auto addr_e = [&] {
        ya = laddr(lds + XBYTES + rq4 * WS + (half - 1) * YTILE + (lane & 3) * YROW + 8 * (4 * (wave & 3) + g));
        PIN(ya);
    };
    auto addr_o = [&] {
        const u32 erow = 256 * (1 + half) + 64 * (wave & 3) + lane;
        ooff = erow * 4096u + (IS_B ? 2048u : 0u) + grp * 16;
        PIN(ooff);
        if constexpr (IS_B) { qoff = erow * 2048u + grp * 16; PIN(qoff); }
    };
    auto addr_s = [&] { sa = laddr(lds + wave * XCH + 112 + 8 * lane); PIN(sa); };
    auto addr_w = [&] { sw = w + SW_OFF + 32 * ((lane & 49) + 2 * rq4 + 4 * (lane & 2)); PIN(sw); };
    auto addr_a = [&] { faa = laddr(w) + SW_OFF + 32 * ((g & 1) + 2 * rq4 + 8 * (g >> 1)) + 8 * (lane & 3); PIN(faa); };
    auto addr_b = [&] { fba = laddr(lds) + EXT_OFF + wave * EXT_A + g * FSTR + 32 - 8 * rq4 + 8 * (lane & 3); PIN(fba); };
    lptr far, g3r, g3w, d3w;
    f32x4 far0, far1;
    auto addr_f = [&] {
        far = lds + EXT_OFF + wave * EXT_A + (4 * (lane & 1) + g) * FSTR + 2 * (16 + (c >> 1) + g);
        d3w = w + KD_OFF + 1024 + 2 * c;
        PIN(far); PIN(d3w);
    };
    auto addr_g = [&] {
        g3r = w + KD_OFF + 1020 + 4 * (lane & 7);
        g3w = w + (lane & 16 ? K1_A : 0) + 2080 + 32 * (lane & 7) + 2 * (lane & 8);
        PIN(g3r); PIN(g3w);
    };
#define AD(f) ST(addr_##f())

    PIN(xw); PIN(pa[0]); PIN(pa[1]); PIN(pa[2]);
    if constexpr (IS_B) {
        addr_t(); addr_x(); addr_y(); addr_c(); addr_e(); addr_o();
        VMWAIT(3);
        SB();
        asm volatile("" : "+v"(h0), "+v"(dd), "+v"(p[0]), "+v"(p[1]), "+v"(p[2]), "+v"(p[3]), "+v"(p[4]), "+v"(p[5]));
        *(LDS u16*)(w + 32) = (u16)pkbf_s(lo16(h0) + lo16(dd), 0.f);
        wave_order();
        u32x4 kc[4];
        u32 kp[4];
#pragma unroll
        for (int i = 0; i < 4; ++i) {
            kc[i] = *(LDS u32x4*)(w + 32 + 1024 * i + 16 * lane);
            kp[i] = *(LDS u32*)(w + 28 + 1024 * i + 16 * lane);
        }
#pragma unroll
        for (int i = 0; i < 4; ++i) *(LDS u32x4*)(w1 + 32 + 1024 * i + 16 * lane) = u32x4{kp[i], kc[i].x, kc[i].y, kc[i].z};
        wave_order();
        w0 = lo16(p[0]); w1f = hi16(p[0]); w2 = lo16(p[1]); w3 = hi16(p[1]);
        SB();
        TR(tl[0][1], 0, 288); TR(th[0][1], 0, 256); TR(tl[0][3], 0, 800); TR(th[0][3], 0, 768);
        TR(tl[0][5], 0, 1312); TR(th[0][5], 0, 1280); TR(tl[0][7], 0, 1824); TR(th[0][7], 0, 1792);
        TR(tl[0][9], 0, 2336); TR(th[0][9], 0, 2304); TR(tl[0][11], 0, 2848); TR(th[0][11], 0, 2816);
        TR(tl[0][13], 0, 3360); TR(th[0][13], 0, 3328); TR(tl[0][15], 0, 3872); TR(th[0][15], 0, 3840);
        TR(tl[1][1], 1, 288); TR(th[1][1], 1, 256); TR(tl[1][3], 1, 800); TR(th[1][3], 1, 768);
        TR(tl[1][5], 1, 1312); TR(th[1][5], 1, 1280); TR(tl[1][7], 1, 1824); TR(th[1][7], 1, 1792);
        TR(tl[1][9], 1, 2336); TR(th[1][9], 1, 2304); TR(tl[1][11], 1, 2848); TR(th[1][11], 1, 2816);
        TR(tl[1][13], 1, 3360); TR(th[1][13], 1, 3328); TR(tl[1][15], 1, 3872); TR(th[1][15], 1, 3840);
        unit_piece(1, 2); unit_piece(1, 0); unit_piece(1, 1);
        VMWAIT(3);
        PK(0, 0); PK(0, 1); PK(0, 2); PV(0, 0); PV(0, 1); LW(0); UK(1); UK(2); UK(3); UK(4); F0(0); F1(0); F2(0);
        F3(0); UV(0); FV(0); UK(5); F0(1); F1(1); F2(1); F3(1); UV(1); FV(1); UK(6); F0(2); F1(2); F2(2); F3(2);
        UV(2); FV(2); UK(7); F0(3); F1(3); F2(3); F3(3); UV(3); FV(3); UK(8); F0(4); F1(4); F2(4); F3(4); UV(4);
        FV(4); PC(0); PX(0, 0); UK(9); F0(5); F1(5); F2(5); F3(5); UV(5); FV(5); PC(1); PX(0, 1); UK(10); F0(6);
        F1(6); F2(6); F3(6); UV(6); FV(6); PC(2); PX(0, 2); UK(11); F0(7); F1(7); F2(7); F3(7); UV(7); FV(7);
        PC(3); PX(0, 3);
    } else {
        u32x4 t1r = touch_srd(a, wave, wave < 6), t2r = touch_srd(a, 8 + wave, true);
        u32 t1o = touch_off(wave), t2o = touch_off(8 + wave);
        GenAddr ga(w, lds + EXT_OFF + wave * EXT_A, lane);
        asm volatile("" : "+s"(t1r), "+s"(t2r));
        PIN(t1o); PIN(t2o); PIN(unit_voff); PIN(pa[3]);
        VMWAIT(1);
        SB();
        asm volatile("" : "+v"(h0), "+v"(dd), "+v"(p[0]), "+v"(p[1]), "+v"(p[2]), "+v"(p[3]), "+v"(p[4]), "+v"(p[5]));
        dma4(t1r, t1o, laddr(lds) + JUNK);
        PRIO(3);
        const float x = lo16(h0), ph = x * 1.44269504088896341f;
        const float pl = fmaf(x, 1.92596299112661746e-8f, fmaf(x, 1.44269504088896341f, -ph));
        const float ex = __builtin_amdgcn_exp2f(ph);
        const float dt = lo16(pkbf(fmaf(ex * 0.69314718055994531f, pl, ex), 0.f));
        gen_kernel_a(ga, lane, p[0], p[1], p[2], p[3], p[4], p[5], dt, lo16(dd),
                     [&] { unit_piece(0, 0); unit_piece(0, 1); },
                     [&] { VMWAIT(0); dma4(t2r, t2o, laddr(lds) + JUNK); PR(0, 0); PR(0, 1); PR(0, 2); PR(0, 3); },
                     [&] { PW(0, 0); PW(0, 1); PW(0, 2); PW(0, 3); },
                     [&](int i) { if (i == 0) AD(t); else if (i == 1) AD(x); else if (i == 2) AD(f); else AD(g); },
                     [&] {
                         TR(tl[0][1], 0, 288); TR(th[0][1], 0, 256); TR(tl[0][3], 0, 800); TR(th[0][3], 0, 768);
                         TR(tl[0][5], 0, 1312); TR(th[0][5], 0, 1280); TR(tl[0][7], 0, 1824);
                         TR(th[0][7], 0, 1792);
                     }, far0, far1);
    }

    u32x4 qv[6];
    auto gate = [&](u32x2 lo, u32x2 hi, u32x4 q) {
        u32x4 y = {lo.x, lo.y, hi.x, hi.y};
        if constexpr (IS_B) {
#pragma unroll
            for (int d = 0; d < 4; ++d) y[d] = pkbf_s(lo16(y[d]) * lo16(q[d]), hi16(y[d]) * hi16(q[d]));
        }
        return y;
    };

    u32 gp[4], gz, ge, gm, go, gn;
    u32x2 g3;
    u32x4 gx;
    u32x2 su[4][3], fa[4][2], fb[4][2];
    u32x4 so[2];
    float sp[32], sd[2], sv;
    f32x4 acc[8][2];
    u32x2 xl[8], xh[8], el, eh;
    u32 p0, p1;
    float c0, k0 = 0.f, v0, o0, o1, o2, o3, ea, eb, ec, ed;

#define XR(d, off) ST(asm volatile("ds_read_b64 %0, %1 offset:%2" : "=v"(d) : "v"(xa), "i"(off) : "memory"))
#define XF(b) u32x4{xl[b].x, xl[b].y, xh[b].x, xh[b].y}
#define TF(s, pi) u32x4{tl[s][pi].x, tl[s][pi].y, th[s][pi].x, th[s][pi].y}
#define MM(j, r, s, pi, b) ST(acc[j][r] = mfma(TF(s, pi), XF(b), acc[j][r]))
#define MZ(j, r, s, pi, b) ST(acc[j][r] = mfma(TF(s, pi), XF(b), f32x4{0.f, 0.f, 0.f, 0.f}))
#define CA(j) ST(c0 = last ? k0 : acc[j][1].w; k0 = acc[j][1].w)
#define BP() ST(asm volatile("ds_bpermute_b32 %0, %1, %2" : "=v"(v0) : "v"(src), "v"(c0) : "memory"))
#define CO0(j) ST(o0 = acc[j][0].x + v0)
#define CO1(j) ST(o1 = acc[j][0].y + acc[j][1].x)
#define CO2(j) ST(o2 = acc[j][0].z + acc[j][1].y)
#define CO3(j) ST(o3 = acc[j][0].w + acc[j][1].z)
#define CC1() ST(p0 = pkbf_s(o0, o1))
#define CC2() ST(p1 = pkbf_s(o2, o3))
#define CW(j) ST(*(LDS u32x2*)(yw + (j) * YTILE) = (u32x2{p0, p1}))
#define GC(i) ST(gp[i] = pkbf(((i) & 2 ? far1 : far0)[2 * ((i) & 1)], ((i) & 2 ? far1 : far0)[2 * ((i) & 1) + 1]))
#define GD() ST(*(LDS u16*)d3w = (u16)gp[0])
#define GR() ST(asm volatile("ds_read2_b32 %0, %1 offset1:1" : "=v"(g3) : "v"(g3r) : "memory"))
#define GZ() ST(gz = c < 13 ? 0u : gp[0])
#define GF(i) ST(*(LDS u16*)(far + 64 * (((i) >> 1) & 1) + 32 * ((i) & 1) + 16 * ((i) >> 2)) = (u16)((i) ? gp[(i) >> 1] >> (16 * ((i) & 1)) : gz))
#define GM0() ST(g3.x = (lane & 7) == 7 ? 0u : g3.x; g3.y &= (lane & 7) == 7 ? 0u : (lane & 7) == 6 ? 0xffffu : ~0u)
#define GM1() ST(ge = perm_hi(g3.x, g3.x); gm = mix(g3.x, g3.y))
#define GM2() ST(go = perm_lo(g3.y, g3.y); gn = perm_hi(g3.y, g3.y))
#define GM3() ST(gx.x = gx.y = lane & 8 ? go : ge; gx.z = lane & 8 ? (lane & 16 ? go : gn) : (lane & 16 ? ge : gm))
#define GM4() ST(gx.w = lane & 8 ? gn : (lane & 16 ? gm : go))
#define GW() ST(*(LDS u32x4*)g3w = gx)
#define SR(ph, b) ST(asm volatile("ds_read_b64 %0, %1 offset:%2" : "=v"(su[ph][b]) : "v"(sa), "i"((ph) * XPH + 8 * (b)) : "memory"))
#define SU(k) ST(sv = HALF(su[(k) & 3][((k) + 12) >> 4], ((k) >> 2) + 3))
#define SK(b) ST(asm volatile("" :: "v"(su[0][b]), "v"(su[1][b]), "v"(su[2][b]), "v"(su[3][b])))
#define SP(k) ST(sp[(k) + 1] = (k) ? sp[k] + sv : sv)
#define SD(t, i) ST(sd[(i) & 1] = (t) ? ((i) ? sp[(i) + 9] - sp[i] : sp[9]) : sp[(i) + 16] - sp[(i) + 9])
#define SC(t, i) ST(so[t][((i) >> 1) & 3] = pkbf_s(sd[0], sd[1]))
#define SW(t, h) ST(*(LDS u32x4*)(sw + (t) * 2048 + 16 * (h)) = so[t])
#define FA(n, t) TRD(fa[n][t], faa, (t) * 2048 + (n) * 512)
#define FB(d, t) TRD(fb[d][t], fba, (t) * 4 * FSTR + 32 * (d))
#define FM(j, n) ST(acc[j][0] = mfma((u32x4{fa[n][1].x, fa[n][1].y, fa[n][0].x, fa[n][0].y}), \
                                     (u32x4{fb[(j) - (n) - 4][1].x, fb[(j) - (n) - 4][1].y, fb[(j) - (n) - 4][0].x, fb[(j) - (n) - 4][0].y}), acc[j][0]))
#define ER(d, s, h) ST(asm volatile("ds_read_b64_tr_b16 %0, %1 offset:%2" : "=v"(d) : "v"(ya), "i"(2 * ((s) - 1) * YTILE + 4 * (h) * WS) : "memory"))
#define EY(d) ((d) < 2 ? el : eh)[(d) & 1]
#define EQ1(s, d) ST(ea = lo16(EY(d)))
#define EQ2(s, d) ST(eb = hi16(EY(d)))
#define EQ3(s, d) ST(ec = lo16(qv[s][d]))
#define EQ4(s, d) ST(ed = hi16(qv[s][d]))
#define EQ5(s, d) ST(ea *= ec)
#define EQ6(s, d) ST(eb *= ed)
#define EQ7(s, d) ST(EY(d) = pkbf_s(ea, eb))
#define ES(s) do { ST(__builtin_amdgcn_raw_buffer_store_b128((u32x4{el.x, el.y, eh.x, eh.y}), ro, (s) == 1 ? ooff + (half ? -512u * 4096u : NOWHERE) : ooff, (s) == 1 ? 0 : ((s) - 2) * 512 * 4096, 0)); \
                   ST(asm volatile("s_nop 1")); } while (0)
#define QL(s) ST(qv[s] = ld128(rq, (s) == 1 ? qoff + (half ? -512u * 2048u : NOWHERE) : (s) == 5 ? qoff + 3u * 512u * 2048u : qoff, (s) == 1 || (s) == 5 ? 0 : ((s) - 2) * 512 * 2048))
#define QW(s, n) ST(asm volatile("s_waitcnt vmcnt(" #n ")" : "+v"(qv[s]) :: "memory"))
#define UL(r, k) ST(unit_piece(r, k))
#define ULA(h, k) ST(dma16_if(half, h, rx0, unit_voff, (1024 + (k) * 64) * 2048, laddr(raw) + RAW + (k) * 1024))
#define VW(n) ST(VMWAIT(n))
#define BAR(i) do { asm volatile("s_barrier" ::: "memory"); SB(); } while (0)

    LW(0);
    if constexpr (IS_B) {
        BAR(0); PRIO(3); XR(xl[0], 1 * XPH + 0); XR(xh[0], 1 * XPH + 0 + 8); XR(xl[1], 3 * XPH + 0);
        XR(xh[1], 3 * XPH + 0 + 8); TR(tl[0][0], 0, 32); TR(th[0][0], 0, 0); LW(4); MZ(0, 1, 0, 1, 0);
        XR(xl[2], 0 * XPH + 0); XR(xh[2], 0 * XPH + 0 + 8); TR(tl[0][2], 0, 544); TR(th[0][2], 0, 512);
        TR(tl[0][4], 0, 1056); MZ(1, 1, 0, 3, 0); TR(th[0][4], 0, 1024); TR(tl[0][6], 0, 1568);
        TR(th[0][6], 0, 1536); TR(tl[0][8], 0, 2080); TR(th[0][8], 0, 2048); MZ(2, 1, 0, 5, 0);
        TR(tl[0][10], 0, 2592); TR(th[0][10], 0, 2560); TR(tl[0][12], 0, 3104); TR(th[0][12], 0, 3072);
        TR(tl[0][14], 0, 3616); MZ(3, 1, 0, 7, 0); TR(th[0][14], 0, 3584); TR(tl[1][0], 1, 32);
        TR(th[1][0], 1, 0); TR(tl[1][2], 1, 544); TR(th[1][2], 1, 512); MZ(4, 1, 0, 9, 0); TR(tl[1][4], 1, 1056);
        TR(th[1][4], 1, 1024); TR(tl[1][6], 1, 1568); TR(th[1][6], 1, 1536); TR(tl[1][8], 1, 2080);
        MZ(5, 1, 0, 11, 0); TR(th[1][8], 1, 2048); TR(tl[1][10], 1, 2592); TR(th[1][10], 1, 2560);
        TR(tl[1][12], 1, 3104); TR(th[1][12], 1, 3072); MZ(6, 1, 0, 13, 0); TR(tl[1][14], 1, 3616);
        TR(th[1][14], 1, 3584); MZ(7, 1, 0, 15, 0); LW(14); MM(0, 1, 1, 1, 1); XR(xl[3], 2 * XPH + 0);
        XR(xh[3], 2 * XPH + 0 + 8); MM(1, 1, 1, 3, 1); MM(2, 1, 1, 5, 1); MM(3, 1, 1, 7, 1); MM(4, 1, 1, 9, 1);
        MM(5, 1, 1, 11, 1); MM(6, 1, 1, 13, 1); MM(7, 1, 1, 15, 1); LW(14); MZ(0, 0, 0, 1, 2);
        XR(xl[0], 1 * XPH + 64); XR(xh[0], 1 * XPH + 64 + 8); MZ(1, 0, 0, 3, 2); MZ(2, 0, 0, 5, 2);
        MZ(3, 0, 0, 7, 2); MZ(4, 0, 0, 9, 2); MZ(5, 0, 0, 11, 2); MZ(6, 0, 0, 13, 2); MZ(7, 0, 0, 15, 2); LW(2);
        MM(0, 0, 1, 1, 3); XR(xl[1], 3 * XPH + 64); XR(xh[1], 3 * XPH + 64 + 8); MM(1, 0, 1, 3, 3);
        MM(2, 0, 1, 5, 3); MM(3, 0, 1, 7, 3); MM(4, 0, 1, 9, 3); MM(5, 0, 1, 11, 3); MM(6, 0, 1, 13, 3);
        MM(7, 0, 1, 15, 3); LW(2); MM(0, 1, 0, 0, 0); XR(xl[2], 0 * XPH + 64); XR(xh[2], 0 * XPH + 64 + 8);
        MM(1, 1, 0, 2, 0); MM(2, 1, 0, 4, 0); MM(3, 1, 0, 6, 0); MM(4, 1, 0, 8, 0); MM(5, 1, 0, 10, 0);
        MM(6, 1, 0, 12, 0); MM(7, 1, 0, 14, 0); QL(1); VW(1); PK(1, 0); PK(1, 1); PK(1, 2); LW(5);
        MM(0, 1, 1, 0, 1); XR(xl[3], 2 * XPH + 64); XR(xh[3], 2 * XPH + 64 + 8); PV(1, 0); PV(1, 1);
        MM(1, 1, 1, 2, 1); MM(2, 1, 1, 4, 1); MM(3, 1, 1, 6, 1); MM(4, 1, 1, 8, 1); MM(5, 1, 1, 10, 1); LW(5);
        UK(1); UK(2); MM(6, 1, 1, 12, 1); UK(3); UK(4); MM(7, 1, 1, 14, 1); F0(0); F1(0); MM(0, 0, 0, 0, 2);
        XR(xl[0], 1 * XPH + 128); XR(xh[0], 1 * XPH + 128 + 8); F2(0); F3(0); MM(1, 0, 0, 2, 2); LW(3); UV(0);
        FV(0); MM(2, 0, 0, 4, 2); PRIO(2); UK(5); F0(1); MM(3, 0, 0, 6, 2); UL(2, 0); F1(1); F2(1);
        MM(4, 0, 0, 8, 2); F3(1); UV(1); MM(5, 0, 0, 10, 2); FV(1); UK(6); MM(6, 0, 0, 12, 2); UL(2, 1); F0(2);
        F1(2); MM(7, 0, 0, 14, 2); F2(2); F3(2); MM(0, 0, 1, 0, 3); XR(xl[1], 3 * XPH + 128);
        XR(xh[1], 3 * XPH + 128 + 8); UV(2); FV(2); MM(1, 0, 1, 2, 3); UL(2, 2); UK(7); F0(3); MM(2, 0, 1, 4, 3);
        F1(3); F2(3); MM(3, 0, 1, 6, 3); F3(3); UV(3); MM(4, 0, 1, 8, 3); FV(3); UK(8); MM(5, 0, 1, 10, 3);
        F0(4); F1(4); MM(6, 0, 1, 12, 3); F2(4); F3(4); MM(7, 0, 1, 14, 3); LW(4); UV(4); FV(4); LW(2);
        MM(1, 1, 0, 1, 0); XR(xl[2], 0 * XPH + 128); XR(xh[2], 0 * XPH + 128 + 8); PC(0); PX(1, 0); UK(9);
        MM(2, 1, 0, 3, 0); F0(5); F1(5); MM(3, 1, 0, 5, 0); F2(5); F3(5); MM(4, 1, 0, 7, 0); UV(5); FV(5);
        MM(5, 1, 0, 9, 0); PC(1); PX(1, 1); UK(10); MM(6, 1, 0, 11, 0); F0(6); F1(6); MM(7, 1, 0, 13, 0); F2(6);
        F3(6); LW(4); MM(1, 1, 1, 1, 1); XR(xl[3], 2 * XPH + 128); XR(xh[3], 2 * XPH + 128 + 8); UV(6); FV(6);
        MM(2, 1, 1, 3, 1); PC(2); PX(1, 2); UK(11); MM(3, 1, 1, 5, 1); F0(7); F1(7); MM(4, 1, 1, 7, 1); F2(7);
        F3(7); MM(5, 1, 1, 9, 1); UV(7); FV(7); MM(6, 1, 1, 11, 1); PRIO(1); PC(3); PX(1, 3); CA(0); BP();
        MM(7, 1, 1, 13, 1); CO1(0); CO2(0); LW(7); MM(1, 0, 0, 1, 2); XR(xl[0], 1 * XPH + 192);
        XR(xh[0], 1 * XPH + 192 + 8); CO3(0); CC2(); MM(2, 0, 0, 3, 2); LW(2); CO0(0); CC1(); CW(0);
        MM(3, 0, 0, 5, 2); MM(4, 0, 0, 7, 2); MM(5, 0, 0, 9, 2); MM(6, 0, 0, 11, 2); MM(7, 0, 0, 13, 2);
        MM(1, 0, 1, 1, 3); XR(xl[1], 3 * XPH + 192); XR(xh[1], 3 * XPH + 192 + 8); MM(2, 0, 1, 3, 3);
        MM(3, 0, 1, 5, 3); MM(4, 0, 1, 7, 3); MM(5, 0, 1, 9, 3); MM(6, 0, 1, 11, 3); MM(7, 0, 1, 13, 3); PRIO(0);
        UL(3, 0); LW(3); MM(1, 1, 0, 0, 0); XR(xl[2], 0 * XPH + 192); XR(xh[2], 0 * XPH + 192 + 8);
        MM(2, 1, 0, 2, 0); MM(3, 1, 0, 4, 0); UL(3, 1); MM(4, 1, 0, 6, 0); MM(5, 1, 0, 8, 0); MM(6, 1, 0, 10, 0);
        UL(3, 2); MM(7, 1, 0, 12, 0); LW(2); MM(1, 1, 1, 0, 1); XR(xl[3], 2 * XPH + 192);
        XR(xh[3], 2 * XPH + 192 + 8); MM(2, 1, 1, 2, 1); MM(3, 1, 1, 4, 1); MM(4, 1, 1, 6, 1);
        BAR(1); PRIO(3); MM(5, 1, 1, 8, 1); MM(6, 1, 1, 10, 1); MM(7, 1, 1, 12, 1); CA(1); BP(); LW(3);
        MM(1, 0, 0, 0, 2); XR(xl[0], 1 * XPH + 256); XR(xh[0], 1 * XPH + 256 + 8); ER(el, 1, 0); ER(eh, 1, 1);
        MM(2, 0, 0, 2, 2); MM(3, 0, 0, 4, 2); MM(4, 0, 0, 6, 2); LW(0); EQ1(1, 0); EQ2(1, 0); MM(5, 0, 0, 8, 2);
        QW(1, 6); EQ3(1, 0); EQ4(1, 0); MM(6, 0, 0, 10, 2); EQ5(1, 0); EQ6(1, 0); MM(7, 0, 0, 12, 2); EQ7(1, 0);
        EQ1(1, 1); MM(1, 0, 1, 0, 3); XR(xl[1], 3 * XPH + 256); XR(xh[1], 3 * XPH + 256 + 8); EQ2(1, 1);
        EQ3(1, 1); MM(2, 0, 1, 2, 3); EQ4(1, 1); EQ5(1, 1); MM(3, 0, 1, 4, 3); EQ6(1, 1); EQ7(1, 1); VW(3);
        PK(2, 0); PK(2, 1); MM(4, 0, 1, 6, 3); EQ1(1, 2); EQ2(1, 2); PK(2, 2); PV(2, 0); PV(2, 1);
        MM(5, 0, 1, 8, 3); EQ3(1, 2); EQ4(1, 2); MM(6, 0, 1, 10, 3); EQ5(1, 2); EQ6(1, 2); MM(7, 0, 1, 12, 3);
        QL(2); EQ7(1, 2); EQ1(1, 3); MM(2, 1, 0, 1, 0); XR(xl[2], 0 * XPH + 256); XR(xh[2], 0 * XPH + 256 + 8);
        EQ2(1, 3); EQ3(1, 3); MM(3, 1, 0, 3, 0); EQ4(1, 3); EQ5(1, 3); MM(4, 1, 0, 5, 0); EQ6(1, 3); EQ7(1, 3);
        ES(1); MM(5, 1, 0, 7, 0); LW(5); UK(1); UK(2); MM(6, 1, 0, 9, 0); UK(3); UK(4); MM(7, 1, 0, 11, 0);
        F0(0); F1(0); MM(2, 1, 1, 1, 1); XR(xl[3], 2 * XPH + 256); XR(xh[3], 2 * XPH + 256 + 8); F2(0); F3(0);
        MM(3, 1, 1, 3, 1); LW(5); UV(0); FV(0); MM(4, 1, 1, 5, 1); UK(5); F0(1); MM(5, 1, 1, 7, 1); F1(1); F2(1);
        MM(6, 1, 1, 9, 1); F3(1); UV(1); MM(7, 1, 1, 11, 1); FV(1); UK(6); LW(2); MM(2, 0, 0, 1, 2);
        XR(xl[0], 1 * XPH + 320); XR(xh[0], 1 * XPH + 320 + 8); F0(2); F1(2); MM(3, 0, 0, 3, 2); F2(2); F3(2);
        MM(4, 0, 0, 5, 2); UV(2); FV(2); MM(5, 0, 0, 7, 2); UK(7); F0(3); MM(6, 0, 0, 9, 2); F1(3); F2(3);
        MM(7, 0, 0, 11, 2); F3(3); UV(3); LW(2); MM(2, 0, 1, 1, 3); XR(xl[1], 3 * XPH + 320);
        XR(xh[1], 3 * XPH + 320 + 8); FV(3); UK(8); MM(3, 0, 1, 3, 3); F0(4); F1(4); MM(4, 0, 1, 5, 3); F2(4);
        F3(4); MM(5, 0, 1, 7, 3); UV(4); FV(4); MM(6, 0, 1, 9, 3); PC(0); PX(2, 0); UK(9); MM(7, 0, 1, 11, 3);
        F0(5); F1(5); LW(3); MM(2, 1, 0, 0, 0); XR(xl[2], 0 * XPH + 320); XR(xh[2], 0 * XPH + 320 + 8); F2(5);
        F3(5); MM(3, 1, 0, 2, 0); UV(5); FV(5); MM(4, 1, 0, 4, 0); PC(1); PX(2, 1); UK(10); MM(5, 1, 0, 6, 0);
        F0(6); F1(6); MM(6, 1, 0, 8, 0); F2(6); F3(6); MM(7, 1, 0, 10, 0); UV(6); FV(6); LW(4);
        MM(2, 1, 1, 0, 1); PRIO(2); XR(xl[3], 2 * XPH + 320); XR(xh[3], 2 * XPH + 320 + 8); PC(2); PX(2, 2);
        UK(11); MM(3, 1, 1, 2, 1); F0(7); F1(7); MM(4, 1, 1, 4, 1); F2(7); F3(7); MM(5, 1, 1, 6, 1); UV(7);
        FV(7); MM(6, 1, 1, 8, 1); PC(3); PX(2, 3); CO1(1); MM(7, 1, 1, 10, 1); CO2(1); CO3(1); LW(5);
        MM(2, 0, 0, 0, 2); XR(xl[0], 1 * XPH + 384); XR(xh[0], 1 * XPH + 384 + 8); CC2(); CO0(1);
        MM(3, 0, 0, 2, 2); CC1(); CW(1); CA(2); BP(); MM(4, 0, 0, 4, 2); MM(5, 0, 0, 6, 2); MM(6, 0, 0, 8, 2);
        MM(7, 0, 0, 10, 2); LW(6); MM(2, 0, 1, 0, 3); XR(xl[1], 3 * XPH + 384); XR(xh[1], 3 * XPH + 384 + 8);
        MM(3, 0, 1, 2, 3); MM(4, 0, 1, 4, 3); MM(5, 0, 1, 6, 3); MM(6, 0, 1, 8, 3); MM(7, 0, 1, 10, 3); QL(3);
        LW(4); MM(3, 1, 0, 1, 0); XR(xl[2], 0 * XPH + 384); XR(xh[2], 0 * XPH + 384 + 8); CO1(2); CO2(2);
        MM(4, 1, 0, 3, 0); CO3(2); CC2(); MM(5, 1, 0, 5, 0); LW(4); CO0(2); CC1(); CW(2); MM(6, 1, 0, 7, 0);
        MM(7, 1, 0, 9, 0); LW(3); MM(3, 1, 1, 1, 1); XR(xl[3], 2 * XPH + 384); XR(xh[3], 2 * XPH + 384 + 8);
        MM(4, 1, 1, 3, 1); PRIO(1); MM(5, 1, 1, 5, 1); MM(6, 1, 1, 7, 1); MM(7, 1, 1, 9, 1); VW(3); PK(3, 0);
        PK(3, 1); PK(3, 2); PV(3, 0); LW(7); MM(3, 0, 0, 1, 2); XR(xl[0], 1 * XPH + 448);
        XR(xh[0], 1 * XPH + 448 + 8); PV(3, 1); MM(4, 0, 0, 3, 2); MM(5, 0, 0, 5, 2); MM(6, 0, 0, 7, 2);
        MM(7, 0, 0, 9, 2); LW(7); MM(3, 0, 1, 1, 3); XR(xl[1], 3 * XPH + 448); XR(xh[1], 3 * XPH + 448 + 8);
        LW(7); UK(1); UK(2); MM(4, 0, 1, 3, 3); UK(3); UK(4); MM(5, 0, 1, 5, 3); F0(0); F1(0); MM(6, 0, 1, 7, 3);
        F2(0); F3(0); MM(7, 0, 1, 9, 3); LW(5); UV(0); FV(0); LW(3); MM(3, 1, 0, 0, 0); XR(xl[2], 0 * XPH + 448);
        XR(xh[2], 0 * XPH + 448 + 8); UK(5); F0(1); MM(4, 1, 0, 2, 0); PRIO(0); F1(1); F2(1); MM(5, 1, 0, 4, 0);
        F3(1); UV(1); MM(6, 1, 0, 6, 0); FV(1); UK(6); MM(7, 1, 0, 8, 0); F0(2); F1(2); LW(2); MM(3, 1, 1, 0, 1);
        XR(xl[3], 2 * XPH + 448); XR(xh[3], 2 * XPH + 448 + 8); F2(2); F3(2); MM(4, 1, 1, 2, 1); UV(2); FV(2);
        MM(5, 1, 1, 4, 1); UK(7); F0(3); MM(6, 1, 1, 6, 1); F1(3); F2(3); MM(7, 1, 1, 8, 1); F3(3); UV(3); LW(2);
        MM(3, 0, 0, 0, 2); FV(3); UK(8); MM(4, 0, 0, 2, 2);
        BAR(2); PRIO(3); XR(xl[0], 1 * XPH + 512); XR(xh[0], 1 * XPH + 512 + 8); XR(xl[1], 3 * XPH + 512);
        XR(xh[1], 3 * XPH + 512 + 8); MM(5, 0, 0, 4, 2); F0(4); F1(4); MM(6, 0, 0, 6, 2); F2(4); F3(4);
        MM(7, 0, 0, 8, 2); XR(xl[2], 0 * XPH + 512); XR(xh[2], 0 * XPH + 512 + 8); UV(4); FV(4); LW(6);
        MM(3, 0, 1, 0, 3); ER(el, 2, 0); ER(eh, 2, 1); PC(0); PX(3, 0); UK(9); MM(4, 0, 1, 2, 3); F0(5); F1(5);
        MM(5, 0, 1, 4, 3); F2(5); F3(5); MM(6, 0, 1, 6, 3); LW(1); EQ1(2, 0); EQ2(2, 0); MM(7, 0, 1, 8, 3);
        QL(4); XR(xl[3], 2 * XPH + 512); XR(xh[3], 2 * XPH + 512 + 8); QW(2, 3); EQ3(2, 0); MM(4, 1, 0, 1, 0);
        XR(xl[4], 1 * XPH + 576); XR(xh[4], 1 * XPH + 576 + 8); EQ4(2, 0); EQ5(2, 0); MM(5, 1, 0, 3, 0);
        EQ6(2, 0); EQ7(2, 0); MM(6, 1, 0, 5, 0); EQ1(2, 1); EQ2(2, 1); MM(7, 1, 0, 7, 0); EQ3(2, 1); EQ4(2, 1);
        MM(4, 1, 1, 1, 1); XR(xl[5], 3 * XPH + 576); XR(xh[5], 3 * XPH + 576 + 8); EQ5(2, 1); EQ6(2, 1);
        MM(5, 1, 1, 3, 1); EQ7(2, 1); EQ1(2, 2); MM(6, 1, 1, 5, 1); EQ2(2, 2); EQ3(2, 2); MM(7, 1, 1, 7, 1);
        EQ4(2, 2); EQ5(2, 2); MM(4, 0, 0, 1, 2); XR(xl[6], 0 * XPH + 576); XR(xh[6], 0 * XPH + 576 + 8);
        EQ6(2, 2); EQ7(2, 2); MM(5, 0, 0, 3, 2); EQ1(2, 3); EQ2(2, 3); MM(6, 0, 0, 5, 2); EQ3(2, 3); EQ4(2, 3);
        MM(7, 0, 0, 7, 2); EQ5(2, 3); EQ6(2, 3); LW(6); MM(4, 0, 1, 1, 3); EQ7(2, 3); ES(2);
        XR(xl[7], 2 * XPH + 576); XR(xh[7], 2 * XPH + 576 + 8); UV(5); MM(5, 0, 1, 3, 3); FV(5); PC(1); PX(3, 1);
        MM(6, 0, 1, 5, 3); UK(10); F0(6); MM(7, 0, 1, 7, 3); F1(6); F2(6); LW(7); MM(4, 1, 0, 0, 4);
        XR(xl[0], 1 * XPH + 640); XR(xh[0], 1 * XPH + 640 + 8); F3(6); UV(6); MM(5, 1, 0, 2, 4); FV(6); PC(2);
        PX(3, 2); MM(6, 1, 0, 4, 4); UK(11); F0(7); MM(7, 1, 0, 6, 4); PRIO(2); F1(7); F2(7); LW(8);
        MM(4, 1, 1, 0, 5); XR(xl[1], 3 * XPH + 640); XR(xh[1], 3 * XPH + 640 + 8); F3(7); UV(7);
        MM(5, 1, 1, 2, 5); FV(7); PC(3); PX(3, 3); MM(6, 1, 1, 4, 5); CA(3); BP(); CO1(3); MM(7, 1, 1, 6, 5);
        CO2(3); CO3(3); LW(10); MM(4, 0, 0, 0, 6); XR(xl[2], 0 * XPH + 640); XR(xh[2], 0 * XPH + 640 + 8); CC2();
        LW(2); CO0(3); MM(5, 0, 0, 2, 6); CC1(); CW(3); MM(6, 0, 0, 4, 6); CA(4); BP(); MM(7, 0, 0, 6, 6);
        MM(4, 0, 1, 0, 7); XR(xl[3], 2 * XPH + 640); XR(xh[3], 2 * XPH + 640 + 8); MM(5, 0, 1, 2, 7);
        MM(6, 0, 1, 4, 7); MM(7, 0, 1, 6, 7); QL(5); MM(5, 1, 0, 1, 0); XR(xl[4], 1 * XPH + 704);
        XR(xh[4], 1 * XPH + 704 + 8); MM(6, 1, 0, 3, 0); PRIO(1); MM(7, 1, 0, 5, 0); CO1(4); CO2(4);
        MM(5, 1, 1, 1, 1); XR(xl[5], 3 * XPH + 704); XR(xh[5], 3 * XPH + 704 + 8); CO3(4); CC2();
        MM(6, 1, 1, 3, 1); LW(6); CO0(4); CC1(); CW(4); MM(7, 1, 1, 5, 1); MM(5, 0, 0, 1, 2);
        XR(xl[6], 0 * XPH + 704); XR(xh[6], 0 * XPH + 704 + 8); MM(6, 0, 0, 3, 2); MM(7, 0, 0, 5, 2); LW(7);
        MM(5, 0, 1, 1, 3); PRIO(0); XR(xl[7], 2 * XPH + 704); XR(xh[7], 2 * XPH + 704 + 8); MM(6, 0, 1, 3, 3);
        MM(7, 0, 1, 5, 3); LW(7); MM(5, 1, 0, 0, 4); MM(6, 1, 0, 2, 4); MM(7, 1, 0, 4, 4); LW(5);
        MM(5, 1, 1, 0, 5); LW(4);
        BAR(3); PRIO(3); XR(xl[0], 1 * XPH + 768); XR(xh[0], 1 * XPH + 768 + 8); XR(xl[1], 3 * XPH + 768);
        MM(6, 1, 1, 2, 5); XR(xh[1], 3 * XPH + 768 + 8); MM(7, 1, 1, 4, 5); LW(6); MM(5, 0, 0, 0, 6);
        XR(xl[2], 0 * XPH + 768); XR(xh[2], 0 * XPH + 768 + 8); MM(6, 0, 0, 2, 6); ER(el, 3, 0); ER(eh, 3, 1);
        MM(7, 0, 0, 4, 6); LW(8); MM(5, 0, 1, 0, 7); XR(xl[3], 2 * XPH + 768); XR(xh[3], 2 * XPH + 768 + 8);
        CA(5); BP(); MM(6, 0, 1, 2, 7); LW(3); EQ1(3, 0); EQ2(3, 0); MM(7, 0, 1, 4, 7); QW(3, 3); EQ3(3, 0);
        EQ4(3, 0); MM(6, 1, 0, 1, 0); XR(xl[4], 1 * XPH + 832); XR(xh[4], 1 * XPH + 832 + 8); EQ5(3, 0);
        EQ6(3, 0); MM(7, 1, 0, 3, 0); EQ7(3, 0); EQ1(3, 1); MM(6, 1, 1, 1, 1); XR(xl[5], 3 * XPH + 832);
        XR(xh[5], 3 * XPH + 832 + 8); EQ2(3, 1); EQ3(3, 1); MM(7, 1, 1, 3, 1); EQ4(3, 1); EQ5(3, 1);
        MM(6, 0, 0, 1, 2); XR(xl[6], 0 * XPH + 832); XR(xh[6], 0 * XPH + 832 + 8); EQ6(3, 1); EQ7(3, 1);
        MM(7, 0, 0, 3, 2); EQ1(3, 2); EQ2(3, 2); LW(7); MM(6, 0, 1, 1, 3); XR(xl[7], 2 * XPH + 832);
        XR(xh[7], 2 * XPH + 832 + 8); EQ3(3, 2); EQ4(3, 2); MM(7, 0, 1, 3, 3); PRIO(2); EQ5(3, 2); EQ6(3, 2);
        LW(6); MM(6, 1, 0, 0, 4); XR(xl[0], 1 * XPH + 896); XR(xh[0], 1 * XPH + 896 + 8); EQ7(3, 2); EQ1(3, 3);
        MM(7, 1, 0, 2, 4); EQ2(3, 3); EQ3(3, 3); LW(6); MM(6, 1, 1, 0, 5); XR(xl[1], 3 * XPH + 896);
        XR(xh[1], 3 * XPH + 896 + 8); EQ4(3, 3); EQ5(3, 3); MM(7, 1, 1, 2, 5); EQ6(3, 3); EQ7(3, 3); ES(3);
        LW(6); MM(6, 0, 0, 0, 6); XR(xl[2], 0 * XPH + 896); XR(xh[2], 0 * XPH + 896 + 8); CO1(5); CO2(5);
        MM(7, 0, 0, 2, 6); CO3(5); CC2(); LW(6); MM(6, 0, 1, 0, 7); XR(xl[3], 2 * XPH + 896);
        XR(xh[3], 2 * XPH + 896 + 8); CO0(5); CC1(); CW(5); MM(7, 0, 1, 2, 7); PRIO(1); LW(7); MM(7, 1, 0, 1, 0);
        XR(xl[4], 1 * XPH + 960); XR(xh[4], 1 * XPH + 960 + 8); CA(6); BP(); LW(8); MM(7, 1, 1, 1, 1);
        XR(xl[5], 3 * XPH + 960); XR(xh[5], 3 * XPH + 960 + 8); LW(8); MM(7, 0, 0, 1, 2);
        XR(xl[6], 0 * XPH + 960); XR(xh[6], 0 * XPH + 960 + 8); LW(8); MM(7, 0, 1, 1, 3); PRIO(0);
        XR(xl[7], 2 * XPH + 960); XR(xh[7], 2 * XPH + 960 + 8); LW(7); MM(7, 1, 0, 0, 4); CO1(6); CO2(6); LW(4);
        MM(7, 1, 1, 0, 5); CO3(6); CC2(); LW(2); MM(7, 0, 0, 0, 6); CO0(6); CC1(); CW(6); LW(1);
        MM(7, 0, 1, 0, 7); LW(0);
        BAR(4); CA(7); BP(); CO1(7); CO2(7); CO3(7); CC2(); LW(0); CO0(7); CC1(); CW(7); ER(el, 4, 0);
        ER(eh, 4, 1); LW(0); EQ1(4, 0); EQ2(4, 0); QW(4, 3); EQ3(4, 0); EQ4(4, 0); EQ5(4, 0); EQ6(4, 0);
        EQ7(4, 0); EQ1(4, 1); EQ2(4, 1); EQ3(4, 1); EQ4(4, 1); EQ5(4, 1); EQ6(4, 1); EQ7(4, 1); EQ1(4, 2);
        EQ2(4, 2); EQ3(4, 2); EQ4(4, 2); EQ5(4, 2); EQ6(4, 2); EQ7(4, 2); EQ1(4, 3); EQ2(4, 3); EQ3(4, 3);
        EQ4(4, 3); EQ5(4, 3); EQ6(4, 3); EQ7(4, 3); ES(4);
        BAR(5); QW(5, 2);
    } else {
        GC(0); GD(); GR(); GC(1); GC(2); GC(3); GZ(); GF(0); GF(1); GF(2); GF(3); GF(4); GF(5); GF(6); GF(7);
        LW(8); GM0(); GM1(); GM2(); GM3(); GM4(); GW(); LW(0);
        BAR(0); PRIO(3); XR(xl[0], 1 * XPH + 0); XR(xh[0], 1 * XPH + 0 + 8); TR(tl[1][1], 1, 288);
        TR(th[1][1], 1, 256); TR(tl[1][3], 1, 800); TR(th[1][3], 1, 768); TR(tl[1][5], 1, 1312);
        TR(th[1][5], 1, 1280); LW(6); MZ(0, 1, 0, 1, 0); TR(tl[1][7], 1, 1824); TR(th[1][7], 1, 1792);
        MZ(1, 1, 0, 3, 0); XR(xl[1], 3 * XPH + 0); XR(xh[1], 3 * XPH + 0 + 8); MZ(2, 1, 0, 5, 0); AD(c);
        XR(xl[2], 0 * XPH + 0); MZ(3, 1, 0, 7, 0); XR(xh[2], 0 * XPH + 0 + 8); TR(tl[0][0], 0, 32); LW(3);
        MM(0, 1, 1, 1, 1); TR(th[0][0], 0, 0); TR(tl[0][2], 0, 544); MM(1, 1, 1, 3, 1); AD(y);
        TR(th[0][2], 0, 512); MM(2, 1, 1, 5, 1); XR(xl[3], 2 * XPH + 0); XR(xh[3], 2 * XPH + 0 + 8);
        MM(3, 1, 1, 7, 1); TR(tl[0][4], 0, 1056); TR(th[0][4], 0, 1024); LW(8); MZ(0, 0, 0, 1, 2); AD(s);
        TR(tl[0][6], 0, 1568); MZ(1, 0, 0, 3, 2); TR(th[0][6], 0, 1536); TR(tl[0][8], 0, 2080);
        MZ(2, 0, 0, 5, 2); TR(th[0][8], 0, 2048); TR(tl[1][8], 1, 2080); MZ(3, 0, 0, 7, 2); AD(w);
        TR(th[1][8], 1, 2048); LW(8); MM(0, 0, 1, 1, 3); TR(tl[1][0], 1, 32); TR(th[1][0], 1, 0);
        MM(1, 0, 1, 3, 3); TR(tl[1][2], 1, 544); TR(th[1][2], 1, 512); MM(2, 0, 1, 5, 3);
        XR(xl[4], 1 * XPH + 64); XR(xh[4], 1 * XPH + 64 + 8); MM(3, 0, 1, 7, 3); AD(o); TR(tl[1][4], 1, 1056);
        LW(1); MM(0, 1, 0, 0, 4); TR(th[1][4], 1, 1024); TR(tl[1][6], 1, 1568); MM(1, 1, 0, 2, 4);
        TR(th[1][6], 1, 1536); AD(e); MM(2, 1, 0, 4, 4); XR(xl[5], 3 * XPH + 64); XR(xh[5], 3 * XPH + 64 + 8);
        MM(3, 1, 0, 6, 4); XR(xl[6], 0 * XPH + 64); XR(xh[6], 0 * XPH + 64 + 8); MZ(4, 1, 0, 8, 4); AD(a);
        XR(xl[7], 2 * XPH + 64); LW(3); MM(0, 1, 1, 0, 5); XR(xh[7], 2 * XPH + 64 + 8); XR(xl[0], 1 * XPH + 128);
        MM(1, 1, 1, 2, 5); XR(xh[0], 1 * XPH + 128 + 8); XR(xl[1], 3 * XPH + 128); MM(2, 1, 1, 4, 5); AD(b);
        XR(xh[1], 3 * XPH + 128 + 8); MM(3, 1, 1, 6, 5); ULA(0, 0); ULA(0, 1); MM(4, 1, 1, 8, 5); PRIO(2);
        SR(0, 0); SR(1, 0); LW(8); MM(0, 0, 0, 0, 6); XR(xl[2], 0 * XPH + 128); XR(xh[2], 0 * XPH + 128 + 8);
        MM(1, 0, 0, 2, 6); CA(0); BP(); MM(2, 0, 0, 4, 6); SR(2, 0); SR(3, 0); MM(3, 0, 0, 6, 6); SR(0, 1);
        SR(1, 1); MZ(4, 0, 0, 8, 6); SR(2, 1); SR(3, 1); LW(14); MM(0, 0, 1, 0, 7); XR(xl[3], 2 * XPH + 128);
        XR(xh[3], 2 * XPH + 128 + 8); MM(1, 0, 1, 2, 7); SR(0, 2); SR(1, 2); MM(2, 0, 1, 4, 7); SR(2, 2);
        SR(3, 2); MM(3, 0, 1, 6, 7); LW(10); SU(0); SK(0); MM(4, 0, 1, 8, 7); SP(0); SU(1); MM(1, 1, 0, 1, 0);
        XR(xl[4], 1 * XPH + 192); XR(xh[4], 1 * XPH + 192 + 8); MM(2, 1, 0, 3, 0); CO1(0); CO2(0);
        MM(3, 1, 0, 5, 0); PRIO(1); CO3(0); CC2(); MM(4, 1, 0, 7, 0); CO0(0); CC1(); MM(1, 1, 1, 1, 1);
        XR(xl[5], 3 * XPH + 192); XR(xh[5], 3 * XPH + 192 + 8); MM(2, 1, 1, 3, 1); CW(0); ULA(1, 0);
        MM(3, 1, 1, 5, 1); ULA(1, 1); SP(1); MM(4, 1, 1, 7, 1); SU(2); SP(2); MM(1, 0, 0, 1, 2);
        XR(xl[6], 0 * XPH + 192); XR(xh[6], 0 * XPH + 192 + 8); MM(2, 0, 0, 3, 2); PRIO(0); SU(3); SP(3);
        MM(3, 0, 0, 5, 2); LW(13); SU(4); MM(4, 0, 0, 7, 2); SP(4); SU(5); LW(11); MM(1, 0, 1, 1, 3);
        XR(xl[7], 2 * XPH + 192); XR(xh[7], 2 * XPH + 192 + 8); MM(2, 0, 1, 3, 3); SP(5); SU(6);
        MM(3, 0, 1, 5, 3); SP(6); SU(7); MM(4, 0, 1, 7, 3); LW(4);
        BAR(1); PRIO(3); MM(1, 1, 0, 0, 4); XR(xl[0], 1 * XPH + 256); XR(xh[0], 1 * XPH + 256 + 8);
        MM(2, 1, 0, 2, 4); SP(7); SU(8); MM(3, 1, 0, 4, 4); SP(8); SU(9); MM(4, 1, 0, 6, 4); ER(el, 1, 0);
        ER(eh, 1, 1); MZ(5, 1, 0, 8, 4); SP(9); SD(1, 0); MM(1, 1, 1, 0, 5); XR(xl[1], 3 * XPH + 256);
        XR(xh[1], 3 * XPH + 256 + 8); MM(2, 1, 1, 2, 5); LW(2); ES(1); MM(3, 1, 1, 4, 5); SD(1, 1); SC(1, 1);
        MM(4, 1, 1, 6, 5); SU(10); SP(10); MM(5, 1, 1, 8, 5); SU(11); SP(11); MM(1, 0, 0, 0, 6);
        XR(xl[2], 0 * XPH + 256); XR(xh[2], 0 * XPH + 256 + 8); MM(2, 0, 0, 2, 6); SD(1, 2); SD(1, 3);
        MM(3, 0, 0, 4, 6); SC(1, 3); SU(12); MM(4, 0, 0, 6, 6); SP(12); SU(13); MZ(5, 0, 0, 8, 6); SP(13);
        SD(1, 4); MM(1, 0, 1, 0, 7); XR(xl[3], 2 * XPH + 256); XR(xh[3], 2 * XPH + 256 + 8); MM(2, 0, 1, 2, 7);
        SD(1, 5); SC(1, 5); MM(3, 0, 1, 4, 7); SU(14); SP(14); MM(4, 0, 1, 6, 7); SU(15); SP(15);
        MM(5, 0, 1, 8, 7); SD(1, 6); SD(1, 7); MM(2, 1, 0, 1, 0); XR(xl[4], 1 * XPH + 320);
        XR(xh[4], 1 * XPH + 320 + 8); MM(3, 1, 0, 3, 0); SC(1, 7); SW(1, 0); MM(4, 1, 0, 5, 0); SU(16); SP(16);
        MM(5, 1, 0, 7, 0); SD(0, 0); SD(0, 1); LW(7); MM(2, 1, 1, 1, 1); XR(xl[5], 3 * XPH + 320);
        XR(xh[5], 3 * XPH + 320 + 8); MM(3, 1, 1, 3, 1); SC(0, 1); SU(17); MM(4, 1, 1, 5, 1); SP(17); SD(1, 8);
        MM(5, 1, 1, 7, 1); SD(1, 9); SC(1, 9); LW(7); MM(2, 0, 0, 1, 2); XR(xl[6], 0 * XPH + 320);
        XR(xh[6], 0 * XPH + 320 + 8); MM(3, 0, 0, 3, 2); SU(18); SP(18); MM(4, 0, 0, 5, 2); SD(0, 2); SD(0, 3);
        MM(5, 0, 0, 7, 2); SC(0, 3); SU(19); LW(7); MM(2, 0, 1, 1, 3); XR(xl[7], 2 * XPH + 320);
        XR(xh[7], 2 * XPH + 320 + 8); MM(3, 0, 1, 3, 3); SP(19); SD(1, 10); MM(4, 0, 1, 5, 3); SD(1, 11);
        SC(1, 11); MM(5, 0, 1, 7, 3); PRIO(2); SU(20); SK(2); SP(20); LW(7); MM(2, 1, 0, 0, 4);
        XR(xl[0], 1 * XPH + 384); XR(xh[0], 1 * XPH + 384 + 8); MM(3, 1, 0, 2, 4); SD(0, 4); SD(0, 5);
        MM(4, 1, 0, 4, 4); SC(0, 5); SU(21); MM(5, 1, 0, 6, 4); SP(21); SD(1, 12); MZ(6, 1, 0, 8, 4); SD(1, 13);
        SC(1, 13); LW(6); MM(2, 1, 1, 0, 5); XR(xl[1], 3 * XPH + 384); XR(xh[1], 3 * XPH + 384 + 8);
        MM(3, 1, 1, 2, 5); SU(22); SP(22); MM(4, 1, 1, 4, 5); SD(0, 6); SD(0, 7); MM(5, 1, 1, 6, 5); SC(0, 7);
        SW(0, 0); MM(6, 1, 1, 8, 5); SU(23); SP(23); LW(7); MM(2, 0, 0, 0, 6); XR(xl[2], 0 * XPH + 384);
        XR(xh[2], 0 * XPH + 384 + 8); MM(3, 0, 0, 2, 6); SD(1, 14); SD(1, 15); MM(4, 0, 0, 4, 6); SC(1, 15);
        SW(1, 1); MM(5, 0, 0, 6, 6); SU(24); SP(24); MZ(6, 0, 0, 8, 6); SD(0, 8); SD(0, 9); LW(8);
        MM(2, 0, 1, 0, 7); XR(xl[3], 2 * XPH + 384); XR(xh[3], 2 * XPH + 384 + 8); MM(3, 0, 1, 2, 7); SC(0, 9);
        SU(25); MM(4, 0, 1, 4, 7); PRIO(1); SP(25); SU(26); MM(5, 0, 1, 6, 7); SP(26); SD(0, 10);
        MM(6, 0, 1, 8, 7); SD(0, 11); SC(0, 11); LW(8); MM(3, 1, 0, 1, 0); SU(27); SP(27); MM(4, 1, 0, 3, 0);
        SU(28); SP(28); MM(5, 1, 0, 5, 0); SD(0, 12); SD(0, 13); MM(6, 1, 0, 7, 0); SC(0, 13); SU(29); LW(6);
        MM(3, 1, 1, 1, 1); SP(29); SU(30); MM(4, 1, 1, 3, 1); SP(30); SD(0, 14); MM(5, 1, 1, 5, 1); SD(0, 15);
        SC(0, 15); MM(6, 1, 1, 7, 1); PRIO(0); SW(0, 1); VW(1); PR(1, 0); LW(5); MM(3, 0, 0, 1, 2); PR(1, 1);
        PR(1, 2); MM(4, 0, 0, 3, 2); PR(1, 3); LW(3); PW(1, 0); MM(5, 0, 0, 5, 2); LW(3); PW(1, 1);
        MM(6, 0, 0, 7, 2); LW(3); PW(1, 2); MM(3, 0, 1, 1, 3); LW(3); PW(1, 3); MM(4, 0, 1, 3, 3);
        XR(xl[4], 1 * XPH + 448); XR(xh[4], 1 * XPH + 448 + 8); MM(5, 0, 1, 5, 3); XR(xl[5], 3 * XPH + 448);
        XR(xh[5], 3 * XPH + 448 + 8); MM(6, 0, 1, 7, 3); XR(xl[6], 0 * XPH + 448); XR(xh[6], 0 * XPH + 448 + 8);
        LW(4); MM(3, 1, 0, 0, 4); XR(xl[7], 2 * XPH + 448); XR(xh[7], 2 * XPH + 448 + 8); MM(4, 1, 0, 2, 4);
        CA(1); BP(); MM(5, 1, 0, 4, 4); CO1(1); CO2(1); MM(6, 1, 0, 6, 4); CO3(1); CC2(); MZ(7, 1, 0, 8, 4);
        LW(0); CO0(1); MM(3, 1, 1, 0, 5); FA(0, 0); FA(0, 1); MM(4, 1, 1, 2, 5); FB(0, 0); FB(0, 1);
        MM(5, 1, 1, 4, 5); CC1(); CW(1); MM(6, 1, 1, 6, 5); CA(2); BP(); MM(7, 1, 1, 8, 5); CO1(2); CO2(2);
        MM(3, 0, 0, 0, 6); CO3(2); CC2(); MM(4, 0, 0, 2, 6); LW(0); CO0(2); MM(5, 0, 0, 4, 6); CC1(); CW(2);
        LW(0);
        BAR(2); PRIO(3); MM(6, 0, 0, 6, 6); XR(xl[0], 1 * XPH + 512); XR(xh[0], 1 * XPH + 512 + 8);
        MZ(7, 0, 0, 8, 6); XR(xl[1], 3 * XPH + 512); XR(xh[1], 3 * XPH + 512 + 8); MM(3, 0, 1, 0, 7);
        XR(xl[2], 0 * XPH + 512); XR(xh[2], 0 * XPH + 512 + 8); MM(4, 0, 1, 2, 7); XR(xl[3], 2 * XPH + 512);
        XR(xh[3], 2 * XPH + 512 + 8); MM(5, 0, 1, 4, 7); ER(el, 2, 0); ER(eh, 2, 1); MM(6, 0, 1, 6, 7); CA(3);
        BP(); MM(7, 0, 1, 8, 7); LW(1); ES(2); FM(4, 0); MM(4, 1, 0, 1, 0); XR(xl[4], 1 * XPH + 576);
        XR(xh[4], 1 * XPH + 576 + 8); MM(5, 1, 0, 3, 0); CO1(3); CO2(3); MM(6, 1, 0, 5, 0); CO3(3); CC2();
        MM(7, 1, 0, 7, 0); LW(2); CO0(3); MM(4, 1, 1, 1, 1); XR(xl[5], 3 * XPH + 576);
        XR(xh[5], 3 * XPH + 576 + 8); MM(5, 1, 1, 3, 1); CC1(); CW(3); MM(6, 1, 1, 5, 1); MM(7, 1, 1, 7, 1);
        MM(4, 0, 0, 1, 2); XR(xl[6], 0 * XPH + 576); XR(xh[6], 0 * XPH + 576 + 8); MM(5, 0, 0, 3, 2);
        MM(6, 0, 0, 5, 2); MM(7, 0, 0, 7, 2); MM(4, 0, 1, 1, 3); XR(xl[7], 2 * XPH + 576);
        XR(xh[7], 2 * XPH + 576 + 8); MM(5, 0, 1, 3, 3); MM(6, 0, 1, 5, 3); MM(7, 0, 1, 7, 3); LW(7);
        MM(4, 1, 0, 0, 4); XR(xl[0], 1 * XPH + 640); XR(xh[0], 1 * XPH + 640 + 8); MM(5, 1, 0, 2, 4); FB(1, 0);
        FB(1, 1); MM(6, 1, 0, 4, 4); PRIO(2); FA(1, 0); FA(1, 1); MM(7, 1, 0, 6, 4); LW(11); MM(4, 1, 1, 0, 5);
        XR(xl[1], 3 * XPH + 640); XR(xh[1], 3 * XPH + 640 + 8); MM(5, 1, 1, 2, 5); MM(6, 1, 1, 4, 5);
        MM(7, 1, 1, 6, 5); LW(10); MM(4, 0, 0, 0, 6); XR(xl[2], 0 * XPH + 640); XR(xh[2], 0 * XPH + 640 + 8);
        MM(5, 0, 0, 2, 6); MM(6, 0, 0, 4, 6); CA(4); BP(); MM(7, 0, 0, 6, 6); LW(11); MM(4, 0, 1, 0, 7);
        XR(xl[3], 2 * XPH + 640); XR(xh[3], 2 * XPH + 640 + 8); MM(5, 0, 1, 2, 7); MM(6, 0, 1, 4, 7);
        MM(7, 0, 1, 6, 7); LW(9); FM(5, 0); PRIO(1); LW(7); FM(5, 1); MM(5, 1, 0, 1, 0);
        XR(xl[4], 1 * XPH + 704); XR(xh[4], 1 * XPH + 704 + 8); MM(6, 1, 0, 3, 0); CO1(4); CO2(4);
        MM(7, 1, 0, 5, 0); CO3(4); CC2(); LW(7); MM(5, 1, 1, 1, 1); XR(xl[5], 3 * XPH + 704);
        XR(xh[5], 3 * XPH + 704 + 8); MM(6, 1, 1, 3, 1); LW(6); CO0(4); MM(7, 1, 1, 5, 1); CC1(); CW(4);
        MM(5, 0, 0, 1, 2); PRIO(0); XR(xl[6], 0 * XPH + 704); XR(xh[6], 0 * XPH + 704 + 8); MM(6, 0, 0, 3, 2);
        MM(7, 0, 0, 5, 2); LW(7); MM(5, 0, 1, 1, 3); FB(2, 0); FB(2, 1); MM(6, 0, 1, 3, 3);
        XR(xl[7], 2 * XPH + 704); XR(xh[7], 2 * XPH + 704 + 8); MM(7, 0, 1, 5, 3); FA(2, 0); FA(2, 1); LW(11);
        MM(5, 1, 0, 0, 4); LW(8);
        BAR(3); PRIO(3); MM(6, 1, 0, 2, 4); XR(xl[0], 1 * XPH + 768); XR(xh[0], 1 * XPH + 768 + 8);
        MM(7, 1, 0, 4, 4); MM(5, 1, 1, 0, 5); XR(xl[1], 3 * XPH + 768); XR(xh[1], 3 * XPH + 768 + 8);
        MM(6, 1, 1, 2, 5); ER(el, 3, 0); ER(eh, 3, 1); MM(7, 1, 1, 4, 5); LW(12); MM(5, 0, 0, 0, 6);
        XR(xl[2], 0 * XPH + 768); XR(xh[2], 0 * XPH + 768 + 8); MM(6, 0, 0, 2, 6); LW(2); ES(3);
        MM(7, 0, 0, 4, 6); MM(5, 0, 1, 0, 7); XR(xl[3], 2 * XPH + 768); XR(xh[3], 2 * XPH + 768 + 8);
        MM(6, 0, 1, 2, 7); CA(5); BP(); MM(7, 0, 1, 4, 7); FM(6, 0); FM(6, 1); FM(6, 2); MM(6, 1, 0, 1, 0);
        XR(xl[4], 1 * XPH + 832); XR(xh[4], 1 * XPH + 832 + 8); MM(7, 1, 0, 3, 0); FB(3, 0); FB(3, 1);
        MM(6, 1, 1, 1, 1); XR(xl[5], 3 * XPH + 832); XR(xh[5], 3 * XPH + 832 + 8); MM(7, 1, 1, 3, 1); CO1(5);
        CO2(5); LW(9); MM(6, 0, 0, 1, 2); XR(xl[6], 0 * XPH + 832); XR(xh[6], 0 * XPH + 832 + 8);
        MM(7, 0, 0, 3, 2); PRIO(2); FA(3, 0); FA(3, 1); LW(11); MM(6, 0, 1, 1, 3); XR(xl[7], 2 * XPH + 832);
        XR(xh[7], 2 * XPH + 832 + 8); MM(7, 0, 1, 3, 3); CO3(5); CC2(); LW(10); MM(6, 1, 0, 0, 4);
        XR(xl[0], 1 * XPH + 896); XR(xh[0], 1 * XPH + 896 + 8); MM(7, 1, 0, 2, 4); CO0(5); CC1(); LW(8);
        MM(6, 1, 1, 0, 5); XR(xl[1], 3 * XPH + 896); XR(xh[1], 3 * XPH + 896 + 8); MM(7, 1, 1, 2, 5); CW(5);
        LW(9); MM(6, 0, 0, 0, 6); XR(xl[2], 0 * XPH + 896); XR(xh[2], 0 * XPH + 896 + 8); MM(7, 0, 0, 2, 6);
        LW(7); MM(6, 0, 1, 0, 7); XR(xl[3], 2 * XPH + 896); XR(xh[3], 2 * XPH + 896 + 8); MM(7, 0, 1, 2, 7);
        PRIO(1); FM(7, 0); CA(6); BP(); FM(7, 1); FM(7, 2); FM(7, 3); LW(8); MM(7, 1, 0, 1, 0);
        XR(xl[4], 1 * XPH + 960); XR(xh[4], 1 * XPH + 960 + 8); LW(8); MM(7, 1, 1, 1, 1); PRIO(0);
        XR(xl[5], 3 * XPH + 960); XR(xh[5], 3 * XPH + 960 + 8); LW(7); MM(7, 0, 0, 1, 2);
        XR(xl[6], 0 * XPH + 960); XR(xh[6], 0 * XPH + 960 + 8); LW(7); MM(7, 0, 1, 1, 3);
        XR(xl[7], 2 * XPH + 960); XR(xh[7], 2 * XPH + 960 + 8); LW(6); MM(7, 1, 0, 0, 4); CO1(6); CO2(6); LW(4);
        MM(7, 1, 1, 0, 5); CO3(6); CC2(); LW(2); MM(7, 0, 0, 0, 6); CO0(6); CC1(); LW(0); MM(7, 0, 1, 0, 7);
        CW(6); LW(0);
        BAR(4); CA(7); BP(); CO1(7); CO2(7); CO3(7); CC2(); LW(0); CO0(7); CC1(); CW(7); ER(el, 4, 0);
        ER(eh, 4, 1); LW(0); ES(4);
        BAR(5);
    }
    if (half == 0) {
        const u32x4 y = gate(tr_read((lptr)(size_t)ya + 8 * YTILE), tr_read((lptr)(size_t)ya + 8 * YTILE + 4 * WS), qv[5]);
        __builtin_amdgcn_raw_buffer_store_b128(y, ro, ooff, 3 * 512 * 4096, 0);
    }
    if constexpr (IS_B) asm volatile(".Lssm_end_b:"); else asm volatile(".Lssm_end_a:");
}

__global__ void __launch_bounds__(512, 2) ssm_conv_kernel(Args) {
    extern __shared__ __attribute__((aligned(16))) char smem[];
    u32x16 ka, kb;
    asm volatile("s_load_dwordx16 %0, %2, 0x0\n\ts_load_dwordx16 %1, %2, 0x40"
                 : "=&s"(ka), "=&s"(kb) : "s"(__builtin_amdgcn_kernarg_segment_ptr()) : "memory");
    lptr lds = (lptr)smem;
    const int slot = wg_slot(), lane = threadIdx.x & 63;
    const int wave = __builtin_amdgcn_readfirstlane(threadIdx.x >> 6);
    const bool is_b = slot < 16;
    const int grp = column((slot >> 3) & 1) * 8 + (slot & 7);
    const int ch = grp * 8 + wave;
    {
        unsigned long long pc;
        u32 len;
        asm volatile(".Lssm_begin:\n\ts_getpc_b64 %0\n\ts_mov_b32 %1, .Lssm_end_a-.Lssm_begin\n\ts_max_u32 %1, %1, .Lssm_end_b-.Lssm_begin"
                     : "=s"(pc), "=s"(len) :: "scc");
        dma4(srd((const void*)(pc & ~127ull), wave == 0 ? len : 0u), lane < 8 ? (slot * 8 + lane) * 128u : NOWHERE, laddr(lds) + JUNK);
    }
    u32 so = ch * 4096 + 16 * lane, ko = ch * 4096, co = ch * 2, to = touch_off(wave);
    u32 po = is_b ? (grp * 8 + 4 * ((lane >> 2) & 1) + (lane & 3)) * 8 : ch * 256 + 4 * lane;
    int kdst = laddr(lds) + XBYTES + wave * WS_B + 32, sel = (slot >> 4) ^ 1;
    PIN(so); PIN(ko); PIN(co); PIN(to); PIN(po);
    asm volatile("" : "+s"(kdst), "+s"(sel));
    SB();
    asm volatile("s_waitcnt lgkmcnt(0)" : "+s"(ka), "+s"(kb) :: "memory");
    auto ptr = [](u32 lo, u32 hi) { return (u16*)(((size_t)hi << 32) | lo); };
    const Args a = {ptr(ka[0], ka[1]),  ptr(ka[2], ka[3]),   ptr(ka[4], ka[5]),   ptr(ka[6], ka[7]),
                    ptr(ka[8], ka[9]),  ptr(ka[10], ka[11]), ptr(ka[12], ka[13]), ptr(ka[14], ka[15]),
                    ptr(kb[0], kb[1]),  ptr(kb[2], kb[3]),   ptr(kb[4], kb[5]),   ptr(kb[6], kb[7]),
                    ptr(kb[8], kb[9]),  ptr(kb[10], kb[11]), ptr(kb[12], kb[13]), ptr(kb[14], kb[15])};
    u32 h0, dd, p[6];
    asm volatile("s_cmp_lg_u32 %[sel], 0\n\t"
                 "s_cbranch_scc1 .Lb%=\n\t"
                 "global_load_ushort %[h0], %[co], %[ldt]\n\t"
                 "global_load_dword %[p0], %[po], %[are]\n\t"
                 "global_load_dword %[p1], %[po], %[aim]\n\t"
                 "global_load_dword %[p2], %[po], %[bre]\n\t"
                 "global_load_dword %[p3], %[po], %[bim]\n\t"
                 "global_load_dword %[p4], %[po], %[cre]\n\t"
                 "global_load_dword %[p5], %[po], %[cim]\n\t"
                 "global_load_ushort %[dd], %[co], %[dsk]\n\t"
                 "s_branch .Le%=\n"
                 ".Lb%=:\n\t"
                 "s_mov_b32 m0, %[dst]\n\t"
                 "global_load_ushort %[h0], %[ko], %[sk]\n\t"
                 "global_load_lds_dwordx4 %[so], %[sk]\n\t"
                 "global_load_lds_dwordx4 %[so], %[sk] offset:1024\n\t"
                 "global_load_lds_dwordx4 %[so], %[sk] offset:2048\n\t"
                 "global_load_lds_dwordx4 %[so], %[sk] offset:3072\n\t"
                 "global_load_dword %[p0], %[po], %[shk]\n\t"
                 "global_load_dword %[p1], %[po], %[shk] offset:4\n\t"
                 "global_load_ushort %[dd], %[co], %[sd]\n"
                 ".Le%=:"
                 : [h0] "=&v"(h0), [dd] "=&v"(dd), [p0] "=&v"(p[0]), [p1] "=&v"(p[1]), [p2] "=&v"(p[2]), [p3] "=&v"(p[3]),
                   [p4] "=&v"(p[4]), [p5] "=&v"(p[5])
                 : [so] "v"(so), [ko] "v"(ko), [co] "v"(co), [po] "v"(po), [sel] "s"(sel), [dst] "s"(kdst), [ldt] "s"(a.log_dt),
                   [are] "s"(a.a_re), [aim] "s"(a.a_im), [bre] "s"(a.b_re), [bim] "s"(a.b_im), [cre] "s"(a.c_re),
                   [cim] "s"(a.c_im), [dsk] "s"(a.dskip), [sk] "s"(a.ssm_k), [shk] "s"(a.shift_k), [sd] "s"(a.ssm_d)
                 : "memory", "scc");
    dma4(touch_srd(a, wave, !is_b && wave >= 6), to, laddr(lds) + JUNK);
    SB();
    if (is_b) run<true>(a, lds, grp, h0, dd, p);
    else run<false>(a, lds, grp, h0, dd, p);
}

extern "C" void kernel_launch(void* const* d_in, const int* in_sizes, int n_in, void* d_out, int out_size, void* d_ws,
                              size_t ws_size, hipStream_t stream) {
    (void)in_sizes; (void)n_in; (void)out_size; (void)d_ws; (void)ws_size;
    Args a;
    a.u = (const u16*)d_in[0];       a.a_re = (const u16*)d_in[1];   a.a_im = (const u16*)d_in[2];
    a.b_re = (const u16*)d_in[3];    a.b_im = (const u16*)d_in[4];   a.c_re = (const u16*)d_in[5];
    a.c_im = (const u16*)d_in[6];    a.log_dt = (const u16*)d_in[7]; a.dskip = (const u16*)d_in[8];
    a.q = (const u16*)d_in[9];       a.k = (const u16*)d_in[10];     a.v = (const u16*)d_in[11];
    a.shift_k = (const u16*)d_in[12]; a.ssm_k = (const u16*)d_in[13]; a.ssm_d = (const u16*)d_in[14];
    a.out = (u16*)d_out;
    (void)hipFuncSetAttribute((const void*)ssm_conv_kernel, hipFuncAttributeMaxDynamicSharedMemorySize, LDS_BYTES);
    ssm_conv_kernel<<<256, 512, LDS_BYTES, stream>>>(a);
}
```
